```python
import torch

def _softmax(x, axis=-1):
    return torch.softmax(x, dim=axis)

def _gelu(x):
    return torch.nn.functional.gelu(x, approximate='tanh')

def _silu(x):
    return torch.nn.functional.silu(x)

def _sigmoid(x):
    return torch.sigmoid(x)

def _layernorm(x, axis=-1, eps=1e-05):
    m = x.mean(dim=axis, keepdim=True)
    v = x.var(dim=axis, keepdim=True, unbiased=False)
    return (x - m) / torch.sqrt(v + eps)

def _scatter_add(tgt, idx, src, axis=0):
    return tgt.clone().index_add_(axis, idx, src)

def _segment_sum(src, seg, n):
    out = torch.zeros((n,) + src.shape[1:], dtype=src.dtype)
    return out.index_add_(0, seg, src)

def _pad(x, pw):
    flat = tuple((v for pair in reversed(pw) for v in pair))
    return torch.nn.functional.pad(x, flat)
kernel_name = 'adaLN_cond__spectral_gfnet__glu_gated_geglu__prompt'

def setup_inputs():

    _w = lambda a: (lambda t: t.to(torch.bfloat16) if t.is_floating_point() else t)(torch.as_tensor(a))

    def _A_setup():
        B, S, D = (1, 2048, 640)
        Df = D // 2 + 1
        return {'x': _w((lambda *s: torch.randn(*s, dtype=torch.bfloat16) * 0.05)(B, S, D)), 'w_r': _w((lambda *s: torch.randn(*s, dtype=torch.bfloat16) * 3.2)(S, Df) * 0.02), 'w_i': _w((lambda *s: torch.randn(*s, dtype=torch.bfloat16) * 3.2)(S, Df) * 0.02)}
    _A_in = _A_setup()

    def _B_setup():
        B, S, Dm, Df = (1, 2048, 640, 1536)
        return {'x': _w((lambda *s: torch.randn(*s, dtype=torch.bfloat16) * 0.05)(B, S, Dm)), 'w_gate': _w((lambda *s: torch.randn(*s, dtype=torch.bfloat16) * 0.4)(Dm, Df) * Dm ** (-0.5)), 'w_up': _w((lambda *s: torch.randn(*s, dtype=torch.bfloat16) * 0.4)(Dm, Df) * Dm ** (-0.5)), 'w_down': _w((lambda *s: torch.randn(*s, dtype=torch.bfloat16) * 0.4)(Df, Dm) * Df ** (-0.5))}
    _B_in = _B_setup()
    return {'A_x': _A_in['x'], 'A_w_r': _A_in['w_r'], 'A_w_i': _A_in['w_i'], 'B_x': _B_in['x'], 'B_w_gate': _B_in['w_gate'], 'B_w_up': _B_in['w_up'], 'B_w_down': _B_in['w_down'], 'c': _w((lambda *s: torch.randn(*s, dtype=torch.bfloat16) * 0.1)(1, 640) * 8.0), 'w_mod': _w((lambda *s: torch.randn(*s, dtype=torch.bfloat16) * 0.1)(640, 6 * 640) * 640 ** (-0.5) * 8.0)}

def reference(A_x, A_w_r, A_w_i, B_x, B_w_gate, B_w_up, B_w_down, c, w_mod):

    def _A_ref(x, w_r, w_i):
        B, S, D = x.shape
        X = (lambda x, axes=(-2, -1): torch.fft.rfft2(x.float(), dim=axes))(x, axes=(-2, -1))
        W = w_r + 1j * w_i
        X = X * W
        y = (lambda x, s, axes=(-2, -1): torch.fft.irfft2(x, s=s, dim=axes).to(torch.bfloat16))(X, (S, D), axes=(-2, -1))
        return y

    def _B_ref(x, w_gate, w_up, w_down):
        g = torch.einsum('bsd,df->bsf', x, w_gate)
        u = torch.einsum('bsd,df->bsf', x, w_up)
        h = _gelu(g) * u
        return torch.einsum('bsf,fd->bsd', h, w_down)
    x = A_x
    mod = (_silu(c) @ w_mod).reshape(c.shape[0], 6, 1, -1)
    sa, ca, ga, sb, cb, gb = (mod[:, 0].clone(), mod[:, 1].clone(), mod[:, 2].clone(), mod[:, 3].clone(), mod[:, 4].clone(), mod[:, 5].clone())
    h = _layernorm(x, axis=-1) * (1 + ca) + sa
    x = x + ga * _A_ref(h, A_w_r, A_w_i)
    h = _layernorm(x, axis=-1) * (1 + cb) + sb
    x = x + gb * _B_ref(h, B_w_gate, B_w_up, B_w_down)
    return x
```

```cpp
#include <hip/hip_runtime.h>
#include <hip/hip_bf16.h>
#include <stdint.h>

typedef _Float16 h8 __attribute__((ext_vector_type(8)));
typedef _Float16 h4 __attribute__((ext_vector_type(4)));
typedef _Float16 h2 __attribute__((ext_vector_type(2)));
typedef __bf16 bf2 __attribute__((ext_vector_type(2)));
typedef __bf16 bf8 __attribute__((ext_vector_type(8)));
typedef float f4 __attribute__((ext_vector_type(4)));
typedef float f2 __attribute__((ext_vector_type(2)));
typedef short s4 __attribute__((ext_vector_type(4)));
typedef unsigned u32;
typedef unsigned u4 __attribute__((ext_vector_type(4)));
typedef unsigned u2 __attribute__((ext_vector_type(2)));
typedef unsigned short u16;
#define LAS __attribute__((address_space(3)))
#define GAS __attribute__((address_space(1)))
#define DI __device__ __forceinline__

constexpr int S = 2048, D = 640, DF = 1536;

constexpr size_t OFF_MOD = 0;
constexpr size_t OFF_WMAX = 8192;
constexpr size_t OFF_SCAL = 8192 + 256;
constexpr size_t OFF_TAB = 16384;
constexpr int T1 = 0, T2 = 6, T64 = 46, T32 = 78, T3 = 86, T4 = 126, TEND = 132;
constexpr size_t OFF_X1T = 262144;
constexpr size_t OFF_WT = OFF_X1T + 320 * 8192;
constexpr size_t OFF_X2T = OFF_WT + 321 * 8192;
constexpr size_t OFF_X1R = OFF_X2T + 320 * 8192;
constexpr size_t OFF_H2 = OFF_X1R + (size_t)S * D * 2;
constexpr size_t OFF_H = OFF_H2 + (size_t)S * D * 2;
constexpr size_t OFF_WGU = OFF_H + (size_t)S * DF * 2;
constexpr size_t OFF_WD = OFF_WGU + (size_t)D * DF * 4;
constexpr size_t WS_END = OFF_WD + (size_t)D * DF * 2;

DI float bflo(u32 v) { return __builtin_bit_cast(float, v << 16); }
DI float bfhi(u32 v) { return __builtin_bit_cast(float, v & 0xffff0000u); }
DI u32 pkbf(float a, float b) { f2 v = {a, b}; return __builtin_bit_cast(u32, __builtin_convertvector(v, bf2)); }
DI u32 pkh(float a, float b) { f2 v = {a, b}; return __builtin_bit_cast(u32, __builtin_convertvector(v, h2)); }
DI float rbf(float a) { return bflo(pkbf(a, a)); }
DI f4 mfma32h(u4 a, u4 b, f4 c) { return __builtin_amdgcn_mfma_f32_16x16x32_f16(__builtin_bit_cast(h8, a), __builtin_bit_cast(h8, b), c, 0, 0, 0); }
DI f4 mfma32b(u4 a, u4 b, f4 c) { return __builtin_amdgcn_mfma_f32_16x16x32_bf16(__builtin_bit_cast(bf8, a), __builtin_bit_cast(bf8, b), c, 0, 0, 0); }
DI u2 trread(const unsigned char* p) { return __builtin_bit_cast(u2, __builtin_amdgcn_ds_read_tr16_b64_v4i16((LAS s4*)p)); }

template <int CTRL> DI float dppf(float x) { return __builtin_bit_cast(float, __builtin_amdgcn_mov_dpp(__builtin_bit_cast(int, x), CTRL, 0xf, 0xf, true)); }
DI float sum16(float x) { x += dppf<0xB1>(x); x += dppf<0x4E>(x); x += dppf<0x141>(x); x += dppf<0x140>(x); return x; }
DI float max16(float x) { x = fmaxf(x, dppf<0xB1>(x)); x = fmaxf(x, dppf<0x4E>(x)); x = fmaxf(x, dppf<0x141>(x)); x = fmaxf(x, dppf<0x140>(x)); return x; }
DI float sum32(float x) {
  x = sum16(x);
  auto s = __builtin_amdgcn_permlane16_swap(__builtin_bit_cast(u32, x), __builtin_bit_cast(u32, x), false, false);
  return __builtin_bit_cast(float, (u32)s[0]) + __builtin_bit_cast(float, (u32)s[1]);
}
DI float max32(float x) {
  x = max16(x);
  auto s = __builtin_amdgcn_permlane16_swap(__builtin_bit_cast(u32, x), __builtin_bit_cast(u32, x), false, false);
  return fmaxf(__builtin_bit_cast(float, (u32)s[0]), __builtin_bit_cast(float, (u32)s[1]));
}
DI float sum64(float x) {
  x = sum32(x);
  auto s = __builtin_amdgcn_permlane32_swap(__builtin_bit_cast(u32, x), __builtin_bit_cast(u32, x), false, false);
  return __builtin_bit_cast(float, (u32)s[0]) + __builtin_bit_cast(float, (u32)s[1]);
}
DI float max64(float x) {
  x = max32(x);
  auto s = __builtin_amdgcn_permlane32_swap(__builtin_bit_cast(u32, x), __builtin_bit_cast(u32, x), false, false);
  return fmaxf(__builtin_bit_cast(float, (u32)s[0]), __builtin_bit_cast(float, (u32)s[1]));
}
DI float pow2_norm(float m, float* inv) {
  u32 e = (__builtin_bit_cast(u32, m) >> 23) & 0xffu;
  e = e > 252u ? 252u : e;
  e = e < 1u ? 1u : e;
  *inv = __builtin_bit_cast(float, (e + 1u) << 23);
  return __builtin_bit_cast(float, (253u - e) << 23);
}
DI float cosr(int num, int den) { return __builtin_amdgcn_cosf((float)(num % den) / (float)den); }
DI float sinr(int num, int den) { return __builtin_amdgcn_sinf((float)(num % den) / (float)den); }

DI int col_freq(int c) {
  if (c == 320) return 320;
  const int sg = c >> 4, rho = c & 15;
  if (sg == 0) return rho < 8 ? 40 * rho : 20 + 40 * (rho - 8);
  return rho < 8 ? sg + 40 * rho : 640 - sg - 40 * rho;
}

DI float tab_value(int fr, int l, int e) {
  const int x = l & 15, g = l >> 4;
  if (fr < T2) {
    const int tj = fr >> 1, j = 16 * tj + x;
    int n1;
    if ((fr & 1) == 0) n1 = e < 4 ? 4 * g + e : 16 + 4 * g + (e - 4);
    else { if (g >= 2 || e >= 4) return 0.f; n1 = 32 + 4 * g + e; }
    if (j >= 40) return 0.f;
    const int sg = j >> 1, c = j & 1;
    float v;
    if (sg == 0) v = c == 0 ? 1.f : ((n1 & 1) ? -1.f : 1.f);
    else v = c == 0 ? cosr(n1 * sg, 40) : -sinr(n1 * sg, 40);
    return 0.25f * v;
  }
  if (fr < T64) {
    const int q = fr - T2, sg = q >> 1, o = q & 1, rho = x, c = g >> 1, n2 = 8 * (g & 1) + e;
    float v;
    if (sg == 0) {
      if (rho == 0) v = c == 0 ? (o == 0 ? 1.f : ((n2 & 1) ? -1.f : 1.f)) : 0.f;
      else if (rho < 8) v = c == 0 ? (o == 0 ? cosr(n2 * rho, 16) : -sinr(n2 * rho, 16)) : 0.f;
      else { const int f = 20 + 40 * (rho - 8); v = c == 1 ? (o == 0 ? cosr(n2 * f, 640) : -sinr(n2 * f, 640)) : 0.f; }
    } else {
      const int ph = sg + 40 * rho;
      if (o == 0) v = c == 0 ? cosr(n2 * ph, 640) : sinr(n2 * ph, 640);
      else { v = c == 0 ? -sinr(n2 * ph, 640) : cosr(n2 * ph, 640); if (rho >= 8) v = -v; }
    }
    return 0.25f * v;
  }
  if (fr < T32) {
    const int q = fr - T64, js = q & 3, o = (q >> 2) & 1, tt = q >> 3;
    const int k1 = 16 * tt + x, n1 = 16 * js + 4 * g + (e >> 1), c = e & 1;
    const float v = o == 0 ? (c == 0 ? cosr(n1 * k1, 64) : sinr(n1 * k1, 64)) : (c == 0 ? -sinr(n1 * k1, 64) : cosr(n1 * k1, 64));
    return 0.125f * v;
  }
  if (fr < T3) {
    const int q = fr - T32, js = q & 1, o = (q >> 1) & 1, t = q >> 2;
    const int k2 = 16 * t + x, n2 = 16 * js + 4 * g + (e >> 1), c = e & 1;
    const float v = o == 0 ? (c == 0 ? cosr(n2 * k2, 32) : sinr(n2 * k2, 32)) : (c == 0 ? -sinr(n2 * k2, 32) : cosr(n2 * k2, 32));
    return 0.125f * v;
  }
  if (fr < T4) {
    const int q = fr - T3, sg = q >> 1, o = q & 1, n2 = x, rho = 4 * g + (e >> 1), c = e & 1;
    float v;
    if (sg == 0) {
      if (o == 0) {
        if (rho == 0) v = c == 0 ? 0.5f : ((n2 & 1) ? -0.5f : 0.5f);
        else if (rho < 8) v = c == 0 ? cosr(n2 * rho, 16) : sinr(n2 * rho, 16);
        else v = 0.f;
      } else {
        if (rho < 8) v = 0.f;
        else { const int f = 20 + 40 * (rho - 8); v = c == 0 ? cosr(n2 * f, 640) : sinr(n2 * f, 640); }
      }
    } else {
      const int ph = sg + 40 * rho;
      const float tau = rho < 8 ? -1.f : 1.f;
      if (o == 0) v = c == 0 ? cosr(n2 * ph, 640) : -tau * sinr(n2 * ph, 640);
      else v = c == 0 ? sinr(n2 * ph, 640) : tau * cosr(n2 * ph, 640);
    }
    return 0.25f * v;
  }
  {
    const int q = fr - T4, tn = q >> 1, n1 = 16 * tn + x;
    int sg;
    if ((q & 1) == 0) sg = 5 * g + (e >> 1);
    else { if (g >= 2 || e >= 4) return 0.f; sg = 5 * (2 * g + (e >> 1)) + 4; }
    if (n1 >= 40) return 0.f;
    const int c = e & 1;
    if (sg == 0) return c == 0 ? 1.f : ((n1 & 1) ? -1.f : 1.f);
    return c == 0 ? cosr(n1 * sg, 40) : -sinr(n1 * sg, 40);
  }
}

constexpr int NB_GEMV = 240, NB_WPRE = 64, NB_WGU = 160, NB_WD = 96, NB_TAB = 33;
constexpr int B_WPRE = NB_GEMV, B_WGU = B_WPRE + NB_WPRE, B_WD = B_WGU + NB_WGU, B_TAB = B_WD + NB_WD, NB_K0 = B_TAB + NB_TAB;

DI void job_gemv(int bid, const u16* __restrict__ cvec, const u16* __restrict__ wmod, unsigned char* __restrict__ ws, unsigned char* sm) {
  const int t = threadIdx.x, half = t & 1, rg = t >> 1;
  const int grp = (bid & 7) * 30 + (bid >> 3), j0 = 16 * grp;
  u4 wv[5]; float sv[5];
#pragma unroll
  for (int i = 0; i < 5; ++i) wv[i] = *(const u4*)(wmod + (size_t)(rg + 128 * i) * 3840 + j0 + 8 * half);
#pragma unroll
  for (int i = 0; i < 5; ++i) {
    const float c = bflo(cvec[rg + 128 * i]);
    sv[i] = rbf(c / (1.f + __expf(-c)));
  }
  float acc[8];
#pragma unroll
  for (int e = 0; e < 8; ++e) acc[e] = 0.f;
#pragma unroll
  for (int i = 0; i < 5; ++i)
#pragma unroll
    for (int e = 0; e < 4; ++e) { acc[2 * e] += sv[i] * bflo(wv[i][e]); acc[2 * e + 1] += sv[i] * bfhi(wv[i][e]); }
  float* red = (float*)sm;
#pragma unroll
  for (int e = 0; e < 8; ++e) red[rg * 17 + 8 * half + e] = acc[e];
  __syncthreads();
  if (t < 64) {
    const int col = t & 15, part = t >> 4;
    float s = 0.f;
#pragma unroll
    for (int i = 0; i < 32; ++i) s += red[(part * 32 + i) * 17 + col];
    s += __shfl_xor(s, 16);
    s += __shfl_xor(s, 32);
    if (t < 16) {
      const int j = j0 + col, sec = j / 640;
      float v = rbf(s);
      if (sec == 1 || sec == 4) v = 1.f + v;
      ((u16*)(ws + OFF_MOD))[j] = (u16)(pkbf(v, v) & 0xffffu);
    }
  }
}

DI void job_wpre(int bid, const u16* __restrict__ wr, const u16* __restrict__ wi, unsigned char* __restrict__ ws, unsigned char* sm) {
  const int t = threadIdx.x, a = bid & 7, q = bid >> 3;
  u32 mx = 0;
#pragma unroll
  for (int it = 0; it < 11; ++it) {
    const int n = t + 256 * it;
    if (n < 8 * 321) {
      const int seg = n / 321, ch = n - seg * 321, arr = seg >> 2, r = seg & 3;
      const size_t row0 = 8 * a + 64 * (4 * q + r);
      const u4 v = *(const u4*)((const unsigned char*)(arr ? wi : wr) + row0 * 642 + ch * 16);
      *(u4*)(sm + seg * 5136 + ch * 16) = v;
#pragma unroll
      for (int e = 0; e < 4; ++e) { mx = max(mx, v[e] & 0x7fffu); mx = max(mx, (v[e] >> 16) & 0x7fffu); }
    }
  }
  __syncthreads();
  const int i = t & 7;
  const int chunk = ((a >> 1) * 2 + (q >> 2)) * 64 + 16 * (q & 3) + 8 * (a & 1) + i;
#pragma unroll
  for (int it = 0; it < 11; ++it) {
    const int c = (t >> 3) + 32 * it;
    if (c < 321) {
      const int f = col_freq(c);
      u4 o;
#pragma unroll
      for (int r = 0; r < 4; ++r) {
        const u32 lo = *(const u16*)(sm + r * 5136 + i * 642 + 2 * f);
        const u32 hi = *(const u16*)(sm + (4 + r) * 5136 + i * 642 + 2 * f);
        o[r] = lo | (hi << 16);
      }
      *(u4*)(ws + OFF_WT + (size_t)c * 8192 + chunk * 16) = o;
    }
  }
  float m = max64(bflo(mx));
  __syncthreads();
  float* red = (float*)sm;
  if ((t & 63) == 0) red[t >> 6] = m;
  __syncthreads();
  if (t == 0) ((float*)(ws + OFF_WMAX))[bid] = fmaxf(fmaxf(red[0], red[1]), fmaxf(red[2], red[3]));
}

DI void job_wgu(int bid, const u16* __restrict__ wg, const u16* __restrict__ wu, unsigned char* __restrict__ ws, unsigned char* sm) {
  const int t = threadIdx.x, nb = bid & 15, kp = bid >> 4;
  constexpr int RS = 400;
#pragma unroll
  for (int it = 0; it < 3; ++it) {
    const int n = t + 256 * it, k = n / 12, ch = n - 12 * k;
    const size_t off = (size_t)(64 * kp + k) * DF + 96 * nb + 8 * ch;
    const u4 gv = *(const u4*)(wg + off), uv = *(const u4*)(wu + off);
    u4 o0 = {gv[0], uv[0], gv[1], uv[1]}, o1 = {gv[2], uv[2], gv[3], uv[3]};
    *(u4*)(sm + k * RS + ch * 32) = o0;
    *(u4*)(sm + k * RS + ch * 32 + 16) = o1;
  }
  __syncthreads();
  const int w = t >> 6, l = t & 63, g = l >> 4, q = (l & 15) >> 2, p = l & 3;
#pragma unroll
  for (int kk = 0; kk < 2; ++kk)
#pragma unroll
    for (int tau = 0; tau < 3; ++tau) {
      const unsigned char* base = sm + (32 * kk + 8 * g + q) * RS + 32 * (3 * w + tau) + 8 * p;
      const u2 lo = trread(base), hi = trread(base + 4 * RS);
      u4 o = {lo[0], lo[1], hi[0], hi[1]};
      *(u4*)(ws + OFF_WGU + (size_t)(((nb * 4 + w) * 20 + 2 * kp + kk) * 3 + tau) * 1024 + l * 16) = o;
    }
}

DI void job_wd(int bid, const u16* __restrict__ wd, unsigned char* __restrict__ ws, unsigned char* sm) {
  const int t = threadIdx.x, hd = bid & 1, kap = bid >> 1;
  constexpr int RS = 656;
#pragma unroll
  for (int it = 0; it < 5; ++it) {
    const int n = t + 256 * it, k = n / 40, ch = n - 40 * k;
    *(u4*)(sm + k * RS + ch * 16) = *(const u4*)(wd + (size_t)(32 * kap + k) * D + 320 * hd + 8 * ch);
  }
  __syncthreads();
  const int w = t >> 6, l = t & 63, g = l >> 4, q = (l & 15) >> 2, p = l & 3;
#pragma unroll
  for (int it = 0; it < 5; ++it) {
    const int i = 5 * w + it, t16 = 20 * hd + i, db = t16 / 5, tau = t16 - 5 * db;
    const unsigned char* base = sm + (8 * g + q) * RS + 32 * i + 8 * p;
    const u2 lo = trread(base), hi = trread(base + 4 * RS);
    u4 o = {lo[0], lo[1], hi[0], hi[1]};
    *(u4*)(ws + OFF_WD + (size_t)(((db * 4 + kap / 12) * 12 + kap % 12) * 5 + tau) * 1024 + l * 16) = o;
  }
}

DI void job_tab(int bid, unsigned char* __restrict__ ws) {
  const int n = bid * 256 + threadIdx.x, fr = n >> 6, l = n & 63;
  u4 o;
#pragma unroll
  for (int e = 0; e < 4; ++e) o[e] = pkh(tab_value(fr, l, 2 * e), tab_value(fr, l, 2 * e + 1));
  *(u4*)(ws + OFF_TAB + (size_t)n * 16) = o;
}

__global__ __launch_bounds__(256) void k0_setup(const u16* __restrict__ cvec, const u16* __restrict__ wmod, const u16* __restrict__ wr,
                                                const u16* __restrict__ wi, const u16* __restrict__ wg, const u16* __restrict__ wu,
                                                const u16* __restrict__ wd, unsigned char* __restrict__ ws) {
  __shared__ __attribute__((aligned(16))) unsigned char sm[8 * 5136];
  const int bid = blockIdx.x;
  if (bid < B_WPRE) job_gemv(bid, cvec, wmod, ws, sm);
  else if (bid < B_WGU) job_wpre(bid - B_WPRE, wr, wi, ws, sm);
  else if (bid < B_WD) job_wgu(bid - B_WGU, wg, wu, ws, sm);
  else if (bid < B_TAB) job_wd(bid - B_WD, wd, ws, sm);
  else job_tab(bid - B_TAB, ws);
}

__global__ __launch_bounds__(256) void k1_rowfft(const u16* __restrict__ x, unsigned char* __restrict__ ws) {
  __shared__ __attribute__((aligned(16))) unsigned char sm[8 * 1280 + 10240];
  unsigned char* himg = sm;
  unsigned char* timg = sm + 8 * 1280;
  const int bid = blockIdx.x, yy = bid >> 3, b = 4 * (bid & 7) + (yy & 3), a = yy >> 2;
  const int t = threadIdx.x, w = t >> 6, l = t & 63, hr = l >> 5, li = l & 31, g = l >> 4;
  const int i = 2 * w + hr, s = 32 * (8 * a + i) + b;
  const u16* mod = (const u16*)(ws + OFF_MOD);
  const unsigned char* tab = ws + OFF_TAB;
  u2 xv[5], sv[5], gv[5];
#pragma unroll
  for (int j = 0; j < 5; ++j) xv[j] = *(const u2*)(x + (size_t)s * D + 128 * j + 4 * li);
#pragma unroll
  for (int j = 0; j < 5; ++j) { sv[j] = *(const u2*)(mod + 128 * j + 4 * li); gv[j] = *(const u2*)(mod + 640 + 128 * j + 4 * li); }
  u4 m1[3], m1b[3], m2[5][2];
#pragma unroll
  for (int tj = 0; tj < 3; ++tj) { m1[tj] = *(const u4*)(tab + (T1 + 2 * tj) * 1024 + l * 16); m1b[tj] = *(const u4*)(tab + (T1 + 2 * tj + 1) * 1024 + l * 16); }
#pragma unroll
  for (int q = 0; q < 5; ++q)
#pragma unroll
    for (int o = 0; o < 2; ++o) m2[q][o] = *(const u4*)(tab + (T2 + 2 * (5 * w + q) + o) * 1024 + l * 16);

  float xf[20];
  float sum = 0.f;
#pragma unroll
  for (int j = 0; j < 5; ++j) {
    xf[4 * j] = bflo(xv[j][0]); xf[4 * j + 1] = bfhi(xv[j][0]); xf[4 * j + 2] = bflo(xv[j][1]); xf[4 * j + 3] = bfhi(xv[j][1]);
    sum += (xf[4 * j] + xf[4 * j + 1]) + (xf[4 * j + 2] + xf[4 * j + 3]);
  }
  u32 mg = 0, ms = 0;
#pragma unroll
  for (int j = 0; j < 5; ++j)
#pragma unroll
    for (int e = 0; e < 2; ++e) {
      mg = max(mg, max(gv[j][e] & 0x7fffu, (gv[j][e] >> 16) & 0x7fffu));
      ms = max(ms, max(sv[j][e] & 0x7fffu, (sv[j][e] >> 16) & 0x7fffu));
    }
  const float mean = sum32(sum) * (1.f / 640.f);
  float ia;
  const float alpha = pow2_norm(max32(bflo(mg)) + max32(bflo(ms)), &ia);
  if (bid == 0 && t == 0) ((float*)(ws + OFF_SCAL))[0] = 0.4f * ia;
  float ss = 0.f;
#pragma unroll
  for (int e = 0; e < 20; ++e) { xf[e] -= mean; ss += xf[e] * xf[e]; }
  const float ra = __builtin_amdgcn_rsqf(sum32(ss) * (1.f / 640.f) + 1e-5f) * alpha;
#pragma unroll
  for (int j = 0; j < 5; ++j) {
    const float h0 = xf[4 * j] * (ra * bflo(gv[j][0])) + alpha * bflo(sv[j][0]);
    const float h1 = xf[4 * j + 1] * (ra * bfhi(gv[j][0])) + alpha * bfhi(sv[j][0]);
    const float h2_ = xf[4 * j + 2] * (ra * bflo(gv[j][1])) + alpha * bflo(sv[j][1]);
    const float h3 = xf[4 * j + 3] * (ra * bfhi(gv[j][1])) + alpha * bfhi(sv[j][1]);
    u2 o = {pkh(h0, h1), pkh(h2_, h3)};
    *(u2*)(himg + i * 1280 + 256 * j + 8 * li) = o;
  }
  __builtin_amdgcn_fence(__ATOMIC_RELEASE, "wavefront");
  __builtin_amdgcn_wave_barrier();
  __builtin_amdgcn_fence(__ATOMIC_ACQUIRE, "wavefront");
#pragma unroll
  for (int rr = 0; rr < 2; ++rr) {
    const int ii = 2 * w + rr;
    const unsigned char* base = himg + ii * 1280;
    const u2 a0 = trread(base + 8 * l), a1 = trread(base + 512 + 8 * l), a2 = trread(base + 1024 + 8 * li);
    const u4 a32 = {a0[0], a0[1], a1[0], a1[1]}, a32b = {a2[0], a2[1], a2[0], a2[1]};
#pragma unroll
    for (int tj = 0; tj < 3; ++tj) {
      f4 acc = {0.f, 0.f, 0.f, 0.f};
      acc = mfma32h(a32, m1[tj], acc);
      acc = mfma32h(a32b, m1b[tj], acc);
      const int j = 16 * tj + (l & 15);
      if (j < 40) {
        u2 o = {pkh(acc[0], acc[1]), pkh(acc[2], acc[3])};
        *(u2*)(timg + ((j * 2 + (g >> 1)) * 8 + (ii ^ (j & 7))) * 16 + (g & 1) * 8) = o;
      }
    }
  }
  __syncthreads();
  const int chunk = ((b >> 4) * 4 + (a >> 1)) * 64 + (b & 15) * 4 + 2 * (a & 1) + g;
#pragma unroll
  for (int q = 0; q < 5; ++q) {
    const int sg = 5 * w + q;
    const u4 av = *(const u4*)(timg + ((sg * 4 + g) * 8 + ((l & 7) ^ ((2 * sg + (g >> 1)) & 7))) * 16);
    const f4 z = {0.f, 0.f, 0.f, 0.f};
    const f4 re = mfma32h(av, m2[q][0], z), im = mfma32h(av, m2[q][1], z);
    if (g < 2) {
      u4 o = {pkh(re[0], im[0]), pkh(re[1], im[1]), pkh(re[2], im[2]), pkh(re[3], im[3])};
      *(u4*)(ws + OFF_X1T + (size_t)(16 * sg + (l & 15)) * 8192 + chunk * 16) = o;
    }
  }
}

__global__ __launch_bounds__(256) void k2_colfft(unsigned char* __restrict__ ws) {
  __shared__ __attribute__((aligned(16))) unsigned char sm[16384];
  const int c = blockIdx.x, cin = c == 320 ? 0 : c;
  const int t = threadIdx.x, w = t >> 6, l = t & 63, x = l & 15, g = l >> 4, tn = w & 1, hk = w >> 1;
  const unsigned char* tab = ws + OFF_TAB;
  u4 av[4];
#pragma unroll
  for (int js = 0; js < 4; ++js) av[js] = *(const u4*)(ws + OFF_X1T + (size_t)cin * 8192 + ((tn * 4 + js) * 64 + x * 4 + g) * 16);
  u4 m64[2][2][4], m32[2][2][2], wt[2];
#pragma unroll
  for (int tt = 0; tt < 2; ++tt)
#pragma unroll
    for (int o = 0; o < 2; ++o)
#pragma unroll
      for (int js = 0; js < 4; ++js) m64[tt][o][js] = *(const u4*)(tab + (T64 + ((hk * 2 + tt) * 2 + o) * 4 + js) * 1024 + l * 16);
#pragma unroll
  for (int tt = 0; tt < 2; ++tt)
#pragma unroll
    for (int o = 0; o < 2; ++o)
#pragma unroll
      for (int js = 0; js < 2; ++js) m32[tt][o][js] = *(const u4*)(tab + (T32 + (tt * 2 + o) * 2 + js) * 1024 + l * 16);
#pragma unroll
  for (int tt = 0; tt < 2; ++tt) wt[tt] = *(const u4*)(ws + OFF_WT + (size_t)c * 8192 + ((w * 2 + tt) * 64 + l) * 16);
  const float wm = ((const float*)(ws + OFF_WMAX))[l];
  float c1[2][4], s1[2][4], c2[2][4], s2[2][4];
#pragma unroll
  for (int tt = 0; tt < 2; ++tt)
#pragma unroll
    for (int r = 0; r < 4; ++r) {
      const float a1 = (float)((16 * tn + 4 * g + r) * (32 * hk + 16 * tt + x)) * (1.f / 2048.f);
      c1[tt][r] = __builtin_amdgcn_cosf(a1); s1[tt][r] = __builtin_amdgcn_sinf(a1);
      const float a2 = (float)((16 * w + 4 * g + r) * (16 * tt + x)) * (1.f / 2048.f);
      c2[tt][r] = __builtin_amdgcn_cosf(a2); s2[tt][r] = __builtin_amdgcn_sinf(a2);
    }
  float ib;
  const float beta = pow2_norm(max64(wm), &ib);
  if (c == 0 && t == 0) ((float*)(ws + OFF_SCAL))[1] = ib;
  if (c == 0) {
#pragma unroll
    for (int js = 0; js < 4; ++js) av[js] &= 0xffffu;
  } else if (c == 320) {
#pragma unroll
    for (int js = 0; js < 4; ++js) av[js] >>= 16;
  }
  const f4 z = {0.f, 0.f, 0.f, 0.f};
#pragma unroll
  for (int tt = 0; tt < 2; ++tt) {
    f4 re = z, im = z;
#pragma unroll
    for (int js = 0; js < 4; ++js) { re = mfma32h(av[js], m64[tt][0][js], re); im = mfma32h(av[js], m64[tt][1][js], im); }
    u4 o;
#pragma unroll
    for (int r = 0; r < 4; ++r) o[r] = pkh(re[r] * c1[tt][r] + im[r] * s1[tt][r], im[r] * c1[tt][r] - re[r] * s1[tt][r]);
    *(u4*)(sm + ((2 * hk + tt) * 2 + tn) * 1024 + l * 16) = o;
  }
  __syncthreads();
  u4 bv[2], yv[2];
#pragma unroll
  for (int js = 0; js < 2; ++js) bv[js] = *(const u4*)(sm + (w * 2 + js) * 1024 + l * 16);
#pragma unroll
  for (int tt = 0; tt < 2; ++tt) {
    f4 re = z, im = z;
#pragma unroll
    for (int js = 0; js < 2; ++js) { re = mfma32h(m32[tt][0][js], bv[js], re); im = mfma32h(m32[tt][1][js], bv[js], im); }
#pragma unroll
    for (int r = 0; r < 4; ++r) {
      const float wr_ = bflo(wt[tt][r]) * beta, wi_ = bfhi(wt[tt][r]) * beta;
      yv[tt][r] = pkh(re[r] * wr_ - im[r] * wi_, -(re[r] * wi_ + im[r] * wr_));
    }
  }
#pragma unroll
  for (int tt = 0; tt < 2; ++tt) {
    f4 re = z, im = z;
#pragma unroll
    for (int js = 0; js < 2; ++js) { re = mfma32h(yv[js], m32[tt][0][js], re); im = mfma32h(yv[js], m32[tt][1][js], im); }
    u4 o;
#pragma unroll
    for (int r = 0; r < 4; ++r) o[r] = pkh(re[r] * c2[tt][r] + im[r] * s2[tt][r], im[r] * c2[tt][r] - re[r] * s2[tt][r]);
    *(u4*)(sm + 8192 + (tt * 4 + w) * 1024 + l * 16) = o;
  }
  __syncthreads();
#pragma unroll
  for (int js = 0; js < 4; ++js) av[js] = *(const u4*)(sm + 8192 + (tn * 4 + js) * 1024 + l * 16);
#pragma unroll
  for (int tt = 0; tt < 2; ++tt) {
    f4 re = z, im = z;
#pragma unroll
    for (int js = 0; js < 4; ++js) { re = mfma32h(av[js], m64[tt][0][js], re); im = mfma32h(av[js], m64[tt][1][js], im); }
    unsigned char* dst = ws + OFF_X2T + (size_t)cin * 8192 + (((hk * 2 + tt) * 2 + tn) * 64 + x * 4 + g) * 16;
    if (c != 0 && c != 320) {
      u4 o = {pkh(re[0], im[0]), pkh(re[1], im[1]), pkh(re[2], im[2]), pkh(re[3], im[3])};
      *(u4*)dst = o;
    } else {
#pragma unroll
      for (int r = 0; r < 4; ++r) *(u16*)(dst + 4 * r + (c == 320 ? 2 : 0)) = (u16)(pkh(re[r], re[r]) & 0xffffu);
    }
  }
}

__global__ __launch_bounds__(256) void k3_rowifft(const u16* __restrict__ x, unsigned char* __restrict__ ws) {
  constexpr int US = 80;
  __shared__ __attribute__((aligned(16))) unsigned char sm[8 * 16 * US];
  const int bid = blockIdx.x, rb = (bid & 7) * 32 + (bid >> 3);
  const int t = threadIdx.x, w = t >> 6, l = t & 63, xx = l & 15, g = l >> 4;
  const unsigned char* tab = ws + OFF_TAB;
  const u16* mod = (const u16*)(ws + OFF_MOD);
  const int n1r = rb >> 2;
  const int roff = (((n1r >> 4) * 2 + ((rb >> 1) & 1)) * 64 + (n1r & 15) * 4 + 2 * (rb & 1)) * 16 + 4 * (l & 7);
  u4 bv[5];
#pragma unroll
  for (int q = 0; q < 5; ++q)
#pragma unroll
    for (int j = 0; j < 4; ++j) bv[q][j] = *(const u32*)(ws + OFF_X2T + (size_t)(16 * (5 * w + q) + 4 * g + j) * 8192 + roff);
  u4 m3[5][2], m4[3], m4b[3];
#pragma unroll
  for (int q = 0; q < 5; ++q)
#pragma unroll
    for (int o = 0; o < 2; ++o) m3[q][o] = *(const u4*)(tab + (T3 + 2 * (5 * w + q) + o) * 1024 + l * 16);
#pragma unroll
  for (int tn = 0; tn < 3; ++tn) { m4[tn] = *(const u4*)(tab + (T4 + 2 * tn) * 1024 + l * 16); m4b[tn] = *(const u4*)(tab + (T4 + 2 * tn + 1) * 1024 + l * 16); }
  u2 xv[2][3], gav[3], sbv[3], cbv[3];
  const bool v2 = xx < 8;
#pragma unroll
  for (int tn = 0; tn < 3; ++tn) {
    const int d = 16 * (16 * tn + (tn == 2 ? (xx & 7) : xx)) + 4 * g;
#pragma unroll
    for (int rr = 0; rr < 2; ++rr) xv[rr][tn] = *(const u2*)(x + (size_t)(8 * rb + 2 * w + rr) * D + d);
    gav[tn] = *(const u2*)(mod + 2 * 640 + d); sbv[tn] = *(const u2*)(mod + 3 * 640 + d); cbv[tn] = *(const u2*)(mod + 4 * 640 + d);
  }
  const float sc0 = ((const float*)(ws + OFF_SCAL))[0], sc1 = ((const float*)(ws + OFF_SCAL))[1];
  const f4 z = {0.f, 0.f, 0.f, 0.f};
  u32 pk[5][4];
#pragma unroll
  for (int q = 0; q < 5; ++q) {
    const f4 re = mfma32h(m3[q][0], bv[q], z), im = mfma32h(m3[q][1], bv[q], z);
#pragma unroll
    for (int r = 0; r < 4; ++r) pk[q][r] = pkh(re[r], im[r]);
  }
  if (xx < 8) {
#pragma unroll
    for (int r = 0; r < 4; ++r) {
      unsigned char* p = sm + (xx * 16 + 4 * g + r) * US;
      u4 o = {pk[0][r], pk[1][r], pk[2][r], pk[3][r]};
      *(u4*)(p + 16 * w) = o;
      *(u32*)(p + 64 + 4 * w) = pk[4][r];
    }
  }
  __syncthreads();
#pragma unroll
  for (int rr = 0; rr < 2; ++rr) {
    const int tok = 2 * w + rr;
    const unsigned char* p = sm + (tok * 16 + xx) * US;
    const u4 a32 = *(const u4*)(p + 16 * g);
    const u2 a16 = *(const u2*)(p + 64 + 8 * (g & 1));
    const u4 a32b = {a16[0], a16[1], a16[0], a16[1]};
    float x1[3][4];
    float sum = 0.f;
#pragma unroll
    for (int tn = 0; tn < 3; ++tn) {
      f4 acc = mfma32h(a32, m4[tn], z);
      acc = mfma32h(a32b, m4b[tn], acc);
      const float xs[4] = {bflo(xv[rr][tn][0]), bfhi(xv[rr][tn][0]), bflo(xv[rr][tn][1]), bfhi(xv[rr][tn][1])};
      const float gs[4] = {bflo(gav[tn][0]), bfhi(gav[tn][0]), bflo(gav[tn][1]), bfhi(gav[tn][1])};
#pragma unroll
      for (int r = 0; r < 4; ++r) {
        const float y = rbf(acc[r] * sc0 * sc1);
        x1[tn][r] = rbf(xs[r] + rbf(gs[r] * y));
        if (tn == 2 && !v2) x1[tn][r] = 0.f;
        sum += x1[tn][r];
      }
    }
    const float mean = sum64(sum) * (1.f / 640.f);
    float ss = 0.f;
#pragma unroll
    for (int tn = 0; tn < 3; ++tn)
#pragma unroll
      for (int r = 0; r < 4; ++r) { const float dd = (tn == 2 && !v2) ? 0.f : x1[tn][r] - mean; ss += dd * dd; }
    const float rs = __builtin_amdgcn_rsqf(sum64(ss) * (1.f / 640.f) + 1e-5f);
#pragma unroll
    for (int tn = 0; tn < 3; ++tn) {
      const float cs[4] = {bflo(cbv[tn][0]), bfhi(cbv[tn][0]), bflo(cbv[tn][1]), bfhi(cbv[tn][1])};
      const float bs[4] = {bflo(sbv[tn][0]), bfhi(sbv[tn][0]), bflo(sbv[tn][1]), bfhi(sbv[tn][1])};
      float hh[4];
#pragma unroll
      for (int r = 0; r < 4; ++r) hh[r] = (x1[tn][r] - mean) * rs * cs[r] + bs[r];
      if (tn < 2 || v2) {
        const size_t off = ((size_t)(8 * rb + tok) * D + 16 * (16 * tn + xx) + 4 * g) * 2;
        u2 o1 = {pkbf(x1[tn][0], x1[tn][1]), pkbf(x1[tn][2], x1[tn][3])};
        u2 o2 = {pkbf(hh[0], hh[1]), pkbf(hh[2], hh[3])};
        *(u2*)(ws + OFF_X1R + off) = o1;
        *(u2*)(ws + OFF_H2 + off) = o2;
      }
    }
  }
}

DI float gelu_tanh(float v) {
  const float zz = v * (1.5957691216f + 0.0713548162f * v * v);
  return v * __builtin_amdgcn_rcpf(1.f + __builtin_amdgcn_exp2f(-1.4426950409f * zz));
}

__global__ __launch_bounds__(256) void g1_simple(unsigned char* __restrict__ ws) {
  const int bid = blockIdx.x, P = bid >> 4, nb = bid & 15;
  const int t = threadIdx.x, w = t >> 6, l = t & 63, xx = l & 15, g = l >> 4;
  f4 acc[8][3];
#pragma unroll
  for (int tt = 0; tt < 8; ++tt)
#pragma unroll
    for (int tau = 0; tau < 3; ++tau) acc[tt][tau] = f4{0.f, 0.f, 0.f, 0.f};
  const unsigned char* wp = ws + OFF_WGU + (size_t)((nb * 4 + w) * 60) * 1024 + l * 16;
  const unsigned char* hp = ws + OFF_H2 + ((size_t)(128 * P + xx) * D + 8 * g) * 2;
  for (int ks = 0; ks < 20; ++ks) {
    u4 wf[3], tf[8];
#pragma unroll
    for (int tau = 0; tau < 3; ++tau) wf[tau] = *(const u4*)(wp + (ks * 3 + tau) * 1024);
#pragma unroll
    for (int tt = 0; tt < 8; ++tt) tf[tt] = *(const u4*)(hp + (size_t)tt * 16 * D * 2 + ks * 64);
#pragma unroll
    for (int tt = 0; tt < 8; ++tt)
#pragma unroll
      for (int tau = 0; tau < 3; ++tau) acc[tt][tau] = mfma32b(wf[tau], tf[tt], acc[tt][tau]);
  }
#pragma unroll
  for (int tt = 0; tt < 8; ++tt)
#pragma unroll
    for (int tau = 0; tau < 3; ++tau) {
      const f4 a = acc[tt][tau];
      const float h0 = rbf(gelu_tanh(rbf(a[0]))) * rbf(a[2]), h1 = rbf(gelu_tanh(rbf(a[1]))) * rbf(a[3]);
      const int o = 3 * w + tau;
      *(u32*)(ws + OFF_H + ((size_t)(8 * P + tt) * 48 + 3 * nb + (o >> 2)) * 1024 + (16 * (o & 3) + xx) * 16 + 4 * g) = pkbf(h0, h1);
    }
}

__global__ __launch_bounds__(256) void g2_simple(unsigned char* __restrict__ ws, u16* __restrict__ out) {
  __shared__ __attribute__((aligned(16))) f4 red[4][20][64];
  const int bid = blockIdx.x, tb = bid >> 3, db = bid & 7;
  const int t = threadIdx.x, w = t >> 6, l = t & 63, xx = l & 15, g = l >> 4;
  f4 acc[4][5];
#pragma unroll
  for (int tt = 0; tt < 4; ++tt)
#pragma unroll
    for (int tau = 0; tau < 5; ++tau) acc[tt][tau] = f4{0.f, 0.f, 0.f, 0.f};
  const unsigned char* wp = ws + OFF_WD + (size_t)((db * 4 + w) * 60) * 1024 + l * 16;
  const unsigned char* hp = ws + OFF_H + ((size_t)(4 * tb) * 48 + 12 * w) * 1024 + l * 16;
  for (int ks = 0; ks < 12; ++ks) {
    u4 wf[5], tf[4];
#pragma unroll
    for (int tau = 0; tau < 5; ++tau) wf[tau] = *(const u4*)(wp + (ks * 5 + tau) * 1024);
#pragma unroll
    for (int tt = 0; tt < 4; ++tt) tf[tt] = *(const u4*)(hp + (size_t)(tt * 48 + ks) * 1024);
#pragma unroll
    for (int tt = 0; tt < 4; ++tt)
#pragma unroll
      for (int tau = 0; tau < 5; ++tau) acc[tt][tau] = mfma32b(wf[tau], tf[tt], acc[tt][tau]);
  }
#pragma unroll
  for (int tt = 0; tt < 4; ++tt)
#pragma unroll
    for (int tau = 0; tau < 5; ++tau) red[w][tt * 5 + tau][l] = acc[tt][tau];
  __syncthreads();
  const u16* mod = (const u16*)(ws + OFF_MOD);
#pragma unroll
  for (int tau = 0; tau < 5; ++tau) {
    const int tt = w;
    const f4 v = (red[0][tt * 5 + tau][l] + red[1][tt * 5 + tau][l]) + (red[2][tt * 5 + tau][l] + red[3][tt * 5 + tau][l]);
    const int d = 80 * db + 16 * tau + 4 * g;
    const size_t off = (size_t)(64 * tb + 16 * tt + xx) * D + d;
    const u2 xv = *(const u2*)(ws + OFF_X1R + off * 2), gv = *(const u2*)(mod + 5 * 640 + d);
    const float o0 = bflo(xv[0]) + rbf(bflo(gv[0]) * rbf(v[0])), o1 = bfhi(xv[0]) + rbf(bfhi(gv[0]) * rbf(v[1]));
    const float o2 = bflo(xv[1]) + rbf(bflo(gv[1]) * rbf(v[2])), o3 = bfhi(xv[1]) + rbf(bfhi(gv[1]) * rbf(v[3]));
    u2 o = {pkbf(o0, o1), pkbf(o2, o3)};
    *(u2*)(out + off) = o;
  }
}

extern "C" void kernel_launch(void* const* d_in, const int* in_sizes, int n_in, void* d_out, int out_size, void* d_ws, size_t ws_size,
                              hipStream_t stream) {
  const u16* x = (const u16*)d_in[0];
  const u16* wr = (const u16*)d_in[1];
  const u16* wi = (const u16*)d_in[2];
  const u16* wg = (const u16*)d_in[4];
  const u16* wu = (const u16*)d_in[5];
  const u16* wd = (const u16*)d_in[6];
  const u16* cv = (const u16*)d_in[7];
  const u16* wm = (const u16*)d_in[8];
  unsigned char* ws = (unsigned char*)d_ws;
  k0_setup<<<NB_K0, 256, 0, stream>>>(cv, wm, wr, wi, wg, wu, wd, ws);
  k1_rowfft<<<256, 256, 0, stream>>>(x, ws);
  k2_colfft<<<321, 256, 0, stream>>>(ws);
  k3_rowifft<<<256, 256, 0, stream>>>(x, ws);
  g1_simple<<<256, 256, 0, stream>>>(ws);
  g2_simple<<<256, 256, 0, stream>>>(ws, (u16*)d_out);
}
```

```cpp
#include <hip/hip_runtime.h>
#include <hip/hip_bf16.h>
#include <stdint.h>

typedef _Float16 h8 __attribute__((ext_vector_type(8)));
typedef _Float16 h2 __attribute__((ext_vector_type(2)));
typedef __bf16 bf2 __attribute__((ext_vector_type(2)));
typedef float f4 __attribute__((ext_vector_type(4)));
typedef float f2 __attribute__((ext_vector_type(2)));
typedef short s4 __attribute__((ext_vector_type(4)));
typedef unsigned u32;
typedef unsigned u4 __attribute__((ext_vector_type(4)));
typedef unsigned u2 __attribute__((ext_vector_type(2)));
typedef unsigned short u16;
typedef unsigned char u8;
#define LAS __attribute__((address_space(3)))
#define GAS __attribute__((address_space(1)))
#define DI __device__ __forceinline__
typedef GAS u32 gu32;
typedef __amdgpu_buffer_rsrc_t rsrc_t;

constexpr int S = 2048, D = 640, DF = 1536;

constexpr int OFF_MODP = 0;
constexpr int OFF_WMAX = 61440;
constexpr int OFF_STAT = 63488;
constexpr int OFF_MODF = 172032;
constexpr int OFF_CNT = 81920;
constexpr int NCNT = 336;
constexpr int OFF_X1T = 196608;
constexpr int OFF_WT = OFF_X1T + 320 * 8192;
constexpr int OFF_X2T = OFF_WT + 321 * 8192;
constexpr int OFF_X1R = OFF_X2T + 320 * 8192;
constexpr int OFF_H2 = OFF_X1R + S * D * 2;
constexpr int OFF_H = OFF_H2 + S * D * 2;
constexpr int OFF_WGU = OFF_H + S * DF * 2;
constexpr int OFF_WD = OFF_WGU + D * DF * 4;
constexpr int WS_END = OFF_WD + D * DF * 2;
constexpr int OOB = 0x40000000;

constexpr int T1 = 0, T2 = 6, T32 = 46, TOD = 54, T3 = 62, T4 = 102, TCOL = 108, TEND = 109;
namespace ct {
constexpr double taylor(double x, bool sine) {
  double term = sine ? x : 1.0, sum = term;
  for (int n = sine ? 2 : 1; n < 24; n += 2) { term = -term * x * x / (double)(n * (n + 1)); sum += term; }
  return sum;
}
constexpr double cos640(int m) {
  if (m > 320) m = 640 - m;
  const bool neg = m > 160;
  if (neg) m = 320 - m;
  const double u = 6.283185307179586476925287 / 640.0;
  const double v = m <= 80 ? taylor(u * m, false) : taylor(u * (160 - m), true);
  return neg ? -v : v;
}
constexpr u16 to_half(double v) {
  const u16 sg = v < 0 ? 0x8000 : 0;
  if (v < 0) v = -v;
  int e = 0;
  while (e > -14 && v < 1.0) { v *= 2.0; --e; }
  const double m = v * 1024.0;
  int q = (int)m;
  const double fr = m - q;
  if (fr > 0.5 || (fr == 0.5 && (q & 1))) ++q;
  return (u16)(sg | (((e + 14) << 10) + q));
}
struct Base { u16 v[3][640]; };
constexpr Base make_base() {
  Base b = {};
  for (int m = 0; m < 640; ++m) { const double c = cos640(m); b.v[0][m] = to_half(c); b.v[1][m] = to_half(0.25 * c); b.v[2][m] = to_half(0.125 * c); }
  return b;
}
constexpr Base base = make_base();
constexpr u16 C(int sc, int m) { return base.v[sc][m % 640]; }
constexpr u16 Sn(int sc, int m) { return base.v[sc][(m + 480) % 640]; }
constexpr u16 ng(u16 v, bool n) { return n ? (u16)(v ^ 0x8000) : v; }
constexpr u16 value(int fr, int l, int e) {
  const int x = l & 15, g = l >> 4;
  if (fr < T2) {
    const int j = 16 * (fr >> 1) + x;
    if (j >= 40 || ((fr & 1) && (g >= 2 || e >= 4))) return 0;
    const int n1 = (fr & 1) ? 32 + 4 * g + e : (e < 4 ? 4 * g + e : 12 + 4 * g + e);
    const int sg = j >> 1;
    if (sg == 0) return ng(C(1, 0), (j & 1) && (n1 & 1));
    return (j & 1) ? ng(Sn(1, 16 * n1 * sg), true) : C(1, 16 * n1 * sg);
  }
  if (fr < T32) {
    const int sg = (fr - T2) >> 1, o = fr & 1, c = g >> 1, n2 = 8 * (g & 1) + e;
    if (sg == 0) {
      if ((x < 8) != (c == 0)) return 0;
      if (x == 0) return ng(C(1, 0), o && (n2 & 1));
      const int f = x < 8 ? 40 * x : 40 * x - 300;
      return o ? ng(Sn(1, n2 * f), true) : C(1, n2 * f);
    }
    const int a = n2 * (sg + 40 * x);
    if (o == 0) return c ? Sn(1, a) : C(1, a);
    return c ? ng(C(1, a), x >= 8) : ng(Sn(1, a), x < 8);
  }
  if (fr < T3) {
    const int q = (fr - T32) & 7, o = (q >> 1) & 1, k = 16 * (q >> 2) + x, m = 16 * (q & 1) + 4 * g + (e >> 1);
    const int a = fr < TOD ? 20 * m * k : 10 * (2 * m + 1) * k;
    if ((e & 1) == o) return C(2, a);
    return ng(Sn(2, a), o == 1);
  }
  if (fr < T4) {
    const int sg = (fr - T3) >> 1, o = fr & 1, rho = 4 * g + (e >> 1), c = e & 1;
    if (sg == 0) {
      if ((rho < 8) != (o == 0)) return 0;
      if (rho == 0) return ng(C(2, 0), c && (x & 1));
      const int f = rho < 8 ? 40 * rho : 40 * rho - 300;
      return c ? Sn(1, x * f) : C(1, x * f);
    }
    const int a = x * (sg + 40 * rho);
    if (o == 0) return c ? ng(Sn(1, a), rho >= 8) : C(1, a);
    return c ? ng(C(1, a), rho < 8) : Sn(1, a);
  }
  {
    const int n1 = 16 * ((fr - T4) >> 1) + x;
    if (n1 >= 40 || ((fr & 1) && (g >= 2 || e >= 4))) return 0;
    const int sg = (fr & 1) ? 5 * (2 * g + (e >> 1)) + 4 : 5 * g + (e >> 1);
    if (sg == 0) return ng(C(0, 0), (e & 1) && (n1 & 1));
    return (e & 1) ? ng(Sn(0, 16 * n1 * sg), true) : C(0, 16 * n1 * sg);
  }
}
template <int N> struct Frags { u16 v[N * 512]; };
template <int A, int N> constexpr Frags<N> make() {
  Frags<N> t = {};
  for (int i = 0; i < N * 512; ++i) t.v[i] = value(A + (i >> 9), (i >> 3) & 63, i & 7);
  return t;
}
constexpr Frags<18> p0 = make<0, 18>(), p1 = make<18, 18>(), p2 = make<36, 18>(), p3 = make<54, 18>(), p4 = make<72, 18>(), p5 = make<90, 18>();
constexpr int col_of(int f) {
  const int j = f / 40, k = f % 40;
  return f == 320 ? 320 : k == 20 ? 8 + j : k < 20 ? 16 * k + j : 655 - 16 * k - j;
}
struct Cols { u16 v[512]; };
constexpr Cols make_cols() {
  Cols t = {};
  for (int i = 0; i < 512; ++i) {
    const int hf = i >> 8, fl = 16 * (2 * (i & 7) + ((i >> 7) & 1)) + ((i >> 3) & 15);
    t.v[i] = fl < 161 - hf ? (u16)col_of(161 * hf + fl) : (u16)0xffff;
  }
  return t;
}
struct alignas(256) All { Frags<18> a, b, c, d, e, f; Cols g; };
}
static __device__ const ct::All g_tab = {ct::p0, ct::p1, ct::p2, ct::p3, ct::p4, ct::p5, ct::make_cols()};

#define STAMP(k, i)
#define STAMPW(k, i)
#define STAMPC(i)
#define QDECL
#define QSTAMP(K)
#define QFLUSH

DI float bflo(u32 v) { return __builtin_bit_cast(float, v << 16); }
DI float bfhi(u32 v) { return __builtin_bit_cast(float, v & 0xffff0000u); }
DI u32 pkbf(float a, float b) { f2 v = {a, b}; return __builtin_bit_cast(u32, __builtin_convertvector(v, bf2)); }
DI u32 pkh(float a, float b) { f2 v = {a, b}; return __builtin_bit_cast(u32, __builtin_convertvector(v, h2)); }
DI float rbf(float a) { return bflo(pkbf(a, a)); }
DI f4 mfma32h(u4 a, u4 b, f4 c) { return __builtin_amdgcn_mfma_f32_16x16x32_f16(__builtin_bit_cast(h8, a), __builtin_bit_cast(h8, b), c, 0, 0, 0); }
DI u2 trread(const u8* p) { return __builtin_bit_cast(u2, __builtin_amdgcn_ds_read_tr16_b64_v4i16((LAS s4*)p)); }
DI rsrc_t mkrs(const void* p, int bytes) { return __builtin_amdgcn_make_buffer_rsrc((void*)p, 0, bytes, 0x00020000); }
#define LD128C(RS, V, SO) __builtin_amdgcn_raw_buffer_load_b128(RS, V, SO, 16)
#define LD64C(RS, V, SO) __builtin_amdgcn_raw_buffer_load_b64(RS, V, SO, 16)
#define LD32C(RS, V, SO) __builtin_amdgcn_raw_buffer_load_b32(RS, V, SO, 16)
#define ST128C(X, RS, V) __builtin_amdgcn_raw_buffer_store_b128(X, RS, V, 0, 16)
#define LD128N(RS, V, SO) __builtin_amdgcn_raw_buffer_load_b128(RS, V, SO, 2)
#define LD128(RS, V, SO) __builtin_amdgcn_raw_buffer_load_b128(RS, V, SO, 0)
#define LD64(RS, V, SO) __builtin_amdgcn_raw_buffer_load_b64(RS, V, SO, 0)
#define LD32(RS, V, SO) __builtin_amdgcn_raw_buffer_load_b32(RS, V, SO, 0)

template <int CTRL> DI float dppf(float x) { return __builtin_bit_cast(float, __builtin_amdgcn_mov_dpp(__builtin_bit_cast(int, x), CTRL, 0xf, 0xf, true)); }
DI float sum16(float x) { x += dppf<0xB1>(x); x += dppf<0x4E>(x); x += dppf<0x141>(x); x += dppf<0x140>(x); return x; }
DI float sum32(float x) {
  x = sum16(x);
  auto s = __builtin_amdgcn_permlane16_swap(__builtin_bit_cast(u32, x), __builtin_bit_cast(u32, x), false, false);
  return __builtin_bit_cast(float, (u32)s[0]) + __builtin_bit_cast(float, (u32)s[1]);
}
DI float sum64(float x) {
  x = sum32(x);
  auto s = __builtin_amdgcn_permlane32_swap(__builtin_bit_cast(u32, x), __builtin_bit_cast(u32, x), false, false);
  return __builtin_bit_cast(float, (u32)s[0]) + __builtin_bit_cast(float, (u32)s[1]);
}
template <int CTRL> DI u32 dppu(u32 x) { return (u32)__builtin_amdgcn_mov_dpp((int)x, CTRL, 0xf, 0xf, true); }
DI u32 umax16(u32 x) { x = max(x, dppu<0xB1>(x)); x = max(x, dppu<0x4E>(x)); x = max(x, dppu<0x141>(x)); x = max(x, dppu<0x140>(x)); return x; }
DI u32 umax64(u32 x) {
  x = umax16(x);
  auto s = __builtin_amdgcn_permlane16_swap(x, x, false, false);
  x = max((u32)s[0], (u32)s[1]);
  auto q = __builtin_amdgcn_permlane32_swap(x, x, false, false);
  return max((u32)q[0], (u32)q[1]);
}
DI float pow2_norm(u32 mbits, float* inv) {
  u32 e = mbits >> 23;
  e = e > 252u ? 252u : e;
  e = e < 1u ? 1u : e;
  *inv = __builtin_bit_cast(float, (e + 1u) << 23);
  return __builtin_bit_cast(float, (253u - e) << 23);
}

#define RLX_AGENT __ATOMIC_RELAXED, __HIP_MEMORY_SCOPE_AGENT
#define SB() __builtin_amdgcn_sched_barrier(0)
#define CBAR() asm volatile("" ::: "memory")
#define VMWAIT(N) do { CBAR(); __builtin_amdgcn_s_waitcnt(((N) & 15) | (((N) >> 4) << 14) | 0x0F70); CBAR(); } while (0)
#define LGKMWAIT0() do { CBAR(); __builtin_amdgcn_s_waitcnt(0xC07F); CBAR(); } while (0)
#define BAR() do { CBAR(); __builtin_amdgcn_s_barrier(); CBAR(); } while (0)
#define DMA_M0(LDSA) asm volatile("s_mov_b32 m0, %0\n\ts_nop 0" ::"s"(LDSA) : "memory", "m0")
#define DMA16(RS, VOFF, SOFF, OFF) asm volatile("buffer_load_dwordx4 %0, %1, %2 offen offset:%3 lds" ::"v"(VOFF), "s"(RS), "s"(SOFF), "n"(OFF) : "memory")
#define DMA16C(RS, VOFF, SOFF, OFF) asm volatile("buffer_load_dwordx4 %0, %1, %2 offen offset:%3 sc1 lds" ::"v"(VOFF), "s"(RS), "s"(SOFF), "n"(OFF) : "memory")
#define DMA16N(RS, VOFF, SOFF, OFF) asm volatile("buffer_load_dwordx4 %0, %1, %2 offen offset:%3 nt lds" ::"v"(VOFF), "s"(RS), "s"(SOFF), "n"(OFF) : "memory")
#define LDSADDR(P) ((u32)(size_t)(LAS u8*)(P))
template <int V> struct IC { static constexpr int v = V; };
template <int I, int N, class F> DI void sfor(F&& f) { if constexpr (I < N) { f(IC<I>{}); sfor<I + 1, N>(f); } }
#define LAM(X) [&](auto X) __attribute__((always_inline))
#define MF_ACC(D, CA, A, CB, B) asm volatile("v_mfma_f32_16x16x32_bf16 %0, %1, %2, %0" : "+v"(D) : CA(A), CB(B))
#define MF_NEW(D, CA, A, CB, B) asm volatile("v_mfma_f32_16x16x32_bf16 %0, %1, %2, 0" : "=&v"(D) : CA(A), CB(B))
#define MH_ACC(D, A, B) asm volatile("v_mfma_f32_16x16x32_f16 %0, %1, %2, %0" : "+v"(D) : "v"(A), "v"(B))
#define MH_NEW(D, A, B) asm volatile("v_mfma_f32_16x16x32_f16 %0, %1, %2, 0" : "=&v"(D) : "v"(A), "v"(B))
#define PIN2_2(A, B) asm volatile("s_nop 1" : "+v"(A), "+v"(B))
#define PIN2_4(A, B, C, E) asm volatile("s_nop 1" : "+v"(A), "+v"(B), "+v"(C), "+v"(E))
#define PIN8_4(A, B, C, E) asm volatile("s_nop 7" : "+v"(A), "+v"(B), "+v"(C), "+v"(E))
#define PIN0_2(A, B) asm volatile("" : "+v"(A), "+v"(B))
#define PIN0_4(A, B, C, E) asm volatile("" : "+v"(A), "+v"(B), "+v"(C), "+v"(E))
DI f4 rbf4(f4 v) { const u32 a = pkbf(v[0], v[1]), b = pkbf(v[2], v[3]); return f4{bflo(a), bfhi(a), bflo(b), bfhi(b)}; }
DI f4 unbf4(u2 v) { return f4{bflo(v[0]), bfhi(v[0]), bflo(v[1]), bfhi(v[1])}; }
DI u32 amax4(f4 v) { const u4 b = __builtin_bit_cast(u4, v) & 0x7fffffffu; return max(max(b[0], b[1]), max(b[2], b[3])); }

DI int k1_row(int j, int i) {
  const int y = j >> 3;
  return 32 * (2 * (16 * ((y >> 2) & 1) + i) + (y >> 3)) + 4 * (j & 7) + (y & 3);
}

DI void job_gemv(int cg, int kq, const u16* __restrict__ cvec, const u16* __restrict__ wmod, u8* __restrict__ ws, u8* sm) {
  const int t = threadIdx.x, w = __builtin_amdgcn_readfirstlane(t >> 6), l = t & 63;
  const rsrc_t rw = mkrs(wmod, D * 3840 * 2), rc = mkrs(cvec, D * 2);
  const int vo = (l >> 3) * 7680 + (l & 7) * 16;
#pragma unroll
  for (int i = 0; i < 5; ++i) {
    DMA_M0(LDSADDR(sm) + (5 * w + i) * 1024);
    DMA16N(rw, vo, (160 * kq + 8 * (5 * w + i)) * 7680 + cg * 128, 0);
  }
  const u32 cv = LD32(rc, t < 80 ? 4 * t : OOB, 320 * kq);
  const float c0 = bflo(cv), c1 = bfhi(cv);
  const u32 sv = pkbf(c0 * __builtin_amdgcn_rcpf(1.f + __builtin_amdgcn_exp2f(-1.4426950409f * c0)),
                      c1 * __builtin_amdgcn_rcpf(1.f + __builtin_amdgcn_exp2f(-1.4426950409f * c1)));
  if (t < 80) *(u32*)(sm + 20480 + 4 * t) = sv;
  VMWAIT(0);
  LGKMWAIT0();
  BAR();
  typedef __bf16 bf8 __attribute__((ext_vector_type(8)));
  f4 acc = {0.f, 0.f, 0.f, 0.f};
  const u8* bp = sm + (8 * (l >> 4) + ((l & 15) >> 2)) * 128 + 32 * w + 8 * (l & 3);
#pragma unroll
  for (int ks = 0; ks < 5; ++ks) {
    const u2 lo = trread(bp + ks * 4096), hi = trread(bp + ks * 4096 + 512);
    const u4 a = *(const u4*)(sm + 20480 + ks * 64 + (l >> 4) * 16), b = {lo[0], lo[1], hi[0], hi[1]};
    acc = __builtin_amdgcn_mfma_f32_16x16x32_bf16(__builtin_bit_cast(bf8, a), __builtin_bit_cast(bf8, b), acc, 0, 0, 0);
  }
  if (l < 16) *(float*)(ws + OFF_MODP + kq * 15360 + (cg * 64 + 16 * w + l) * 4) = acc[0];
}

DI void job_stats(int j, const u16* __restrict__ x, u8* __restrict__ ws) {
  const int t = threadIdx.x, li = t & 31, i = t >> 5;
  const u16* xr = x + (size_t)k1_row(j & 127, 8 * (j >> 7) + i) * D + 4 * li;
  u2 xv[5];
#pragma unroll
  for (int q = 0; q < 5; ++q) xv[q] = *(const u2*)(xr + 128 * q);
  f4 xf[5];
#pragma unroll
  for (int q = 0; q < 5; ++q) xf[q] = unbf4(xv[q]);
  const f4 s4_ = ((xf[0] + xf[1]) + (xf[2] + xf[3])) + xf[4];
  const float mean = sum32((s4_[0] + s4_[1]) + (s4_[2] + s4_[3])) * (1.f / 640.f);
  f4 q4 = {0.f, 0.f, 0.f, 0.f};
#pragma unroll
  for (int q = 0; q < 5; ++q) { xf[q] -= mean; q4 += xf[q] * xf[q]; }
  const float rs = __builtin_amdgcn_rsqf(sum32((q4[0] + q4[1]) + (q4[2] + q4[3])) * (1.f / 640.f) + 1e-5f);
  if (li == 0) *(f2*)(ws + OFF_STAT + ((j & 127) * 16 + 8 * (j >> 7) + i) * 8) = f2{mean, rs};
}

constexpr int K0_STATS = 80, K0_REST = 336, K0_TAB = 496, K0_GRID = 528;
extern "C" __global__ __launch_bounds__(256, 2) void k0_setup(const u16* __restrict__ x, const u16* __restrict__ cvec, const u16* __restrict__ wmod,
                                                              u8* __restrict__ ws) {
  __shared__ __attribute__((aligned(16))) u8 sm[20800];
  const int bid = blockIdx.x;
  STAMP(0, 0);
  if (bid < K0_STATS) job_gemv(bid % 20, bid / 20, cvec, wmod, ws, sm);
  else if (bid < K0_REST) job_stats(bid - K0_STATS, x, ws);
  else if (bid < K0_TAB) job_gemv(20 + (bid - K0_REST) % 40, (bid - K0_REST) / 40, cvec, wmod, ws, sm);
  else {
    const int n = (bid - K0_TAB) * 256 + threadIdx.x;
    if (n < NCNT) *(u32*)(ws + OFF_CNT + n * 256) = 0u;
    if (n >= 512 && n < 640) *(u4*)(ws + OFF_WMAX + (n - 512) * 16) = u4{0u, 0u, 0u, 0u};
    const rsrc_t rt = mkrs(&g_tab, TEND * 1024);
    u4 a = {0u, 0u, 0u, 0u};
#pragma unroll
    for (int i = 0; i < 7; ++i) a ^= LD128(rt, threadIdx.x * 16 + i * 4096, ((bid - K0_TAB) >> 3) * 28672);
    asm volatile("" :: "v"(a));
    unsigned long long pc; int da, len;
    asm volatile("s_getpc_b64 %0\n.Lcp%=:\n\ts_mov_b32 %1, kmain-.Lcp%=\n\ts_mov_b32 %2, kz_end-kmain" : "=s"(pc), "=s"(da), "=s"(len));
    const rsrc_t rc = mkrs((const void*)(pc + (long long)da), len);
    u4 b = {0u, 0u, 0u, 0u};
#pragma unroll
    for (int i = 0; i < 3; ++i) b ^= LD128(rc, threadIdx.x * 16 + i * 4096, ((bid - K0_TAB) >> 3) * 12288);
    asm volatile("" :: "v"(b));
  }
  STAMPW(0, bid < K0_STATS ? 1 : bid < K0_REST ? 2 : bid < K0_TAB ? 3 : 4);
}

DI void wait_cnt(gu32* p, u32 want) {
  for (u32 spins = 0; spins < (1u << 20); ++spins) {
    if ((u32)__builtin_amdgcn_readfirstlane(__hip_atomic_load(p, RLX_AGENT)) >= want) break;
  }
  __builtin_amdgcn_fence(__ATOMIC_ACQUIRE, "wavefront");
}
DI void wait_edge(gu32* p) {
  for (u32 spins = 0; spins < (1u << 20); ++spins) {
    if (__builtin_amdgcn_ballot_w64(__hip_atomic_load(p, RLX_AGENT) < 16u) == 0ull) break;
  }
  __builtin_amdgcn_fence(__ATOMIC_ACQUIRE, "wavefront");
}
#define ATOMIC_INC(WV, P) do { if (w == (WV)) asm volatile("s_mov_b64 exec, 1\n\tglobal_atomic_add %0, %1, %2\n\ts_mov_b64 exec, -1" :: "v"(0), "v"(1), "s"(P) : "memory"); } while (0)
#define EDGE_SIGNAL(P) do { if (w == 0) asm volatile("s_mov_b64 exec, 0xff\n\tglobal_atomic_add %0, %1, %2\n\ts_mov_b64 exec, -1" :: "v"(l * 4096), "v"(1), "s"(P) : "memory"); } while (0)
DI u4 cmul_pack(f4 re, f4 im, f4 c, f4 s) {
  const f4 a = re * c + im * s, b = im * c - re * s;
  return u4{pkh(a[0], b[0]), pkh(a[1], b[1]), pkh(a[2], b[2]), pkh(a[3], b[3])};
}
struct Wts { const u16 *wg, *wu, *wd, *wr, *wi; };
#define PUB0 45
#define PUB1 65
#define PI0 63
#define PC0 72
#define PI1 74
#define PC1 1
#define PUB2 4
#define PI2 9
#define PC2 16
#define PUB3 19
#define PW3 29
constexpr int W2V = 40;
constexpr int NPRE = W2V + 4;
constexpr int seq15(int m, int b) { return (b == 1 && (m & 1)) ? -1 : (3 * m + 1) / 2 + b; }
constexpr int g1_w2(int st, int b) {
  if (st >= 20 && st < 60) { const int ks = st % 20; return b == 0 ? st - 20 : (ks >= 8 && ks < 8 + (NPRE - 40) / 2) ? 40 + (st / 40) * ((NPRE - 40) / 2) + ks - 8 : -1; }
  if (st >= 61 && st < PC0) { const int i = seq15(st - 61, b); return i >= 0 && NPRE + i < 60 ? NPRE + i : -1; }
  return -1;
}
constexpr int hsel(int k, int k0, int b) { if (k < k0 || k >= k0 + 8) return -1; return seq15(k - k0, b); }
constexpr int g1_epi(int st, int j) { if (st < 20 || j == 0 || j == 3) return -1; const int e = (st % 20) * 4 + (j < 3 ? j - 1 : j - 2); return e < 67 ? e : -1; }
constexpr int g2_epi(int i, int b) { return (b == 0 && i < 67) ? i : -1; }
constexpr bool epi_store(int e) { return e > 0 && (e % 11 == 0); }
constexpr int nvm(int n) {
  if (n < 480) {
    const int st = n / 6, j = n % 6;
    if (j == 0) return (st < 20) + (g1_w2(st, 0) >= 0) + (g1_w2(st, 1) >= 0) + (hsel(st, PC0, 0) >= 0) + (hsel(st, PC0, 1) >= 0);
    return epi_store(g1_epi(st, j)) + (j == 1 && (st == PI0 || st == PI1));
  }
  const int i = n - 480, k = i / 5, tau = i % 5;
  int c = epi_store(g2_epi(i, 0)) + epi_store(g2_epi(i, 1));
  if (tau == 1) c += (hsel(k, PC1, 0) >= 0) + (hsel(k, PC1, 1) >= 0) + (hsel(k, PC2, 0) >= 0) + (hsel(k, PC2, 1) >= 0);
  if (tau == 3 && k == PI2) ++c;
  return c;
}
constexpr int vm_between(int a, int b) { int c = 0; for (int n = a + 1; n < b; ++n) c += nvm(n); return c > 63 ? 63 : c; }
constexpr int last_store(int gp) { return gp < 3 ? 6 * (20 * (gp + 1) + 16) + 4 : 480 + 66; }
static_assert(g1_epi(36, 4) == 66 && g1_w2(PC0 - 1, 0) + (g1_w2(PC0 - 1, 1) >= 0) >= 59 - (g1_w2(PC0 - 1, 0) < 0 ? 60 : 0), "schedule");

DI void stage_pair(bool jg, int jb, const Wts& p, u32 lds, int w, int l) {
  const rsrc_t ra = mkrs(jg ? p.wg : p.wr, jg ? D * DF * 2 : S * 321 * 2), rb = mkrs(jg ? p.wu : p.wi, jg ? D * DF * 2 : S * 321 * 2);
#pragma unroll
  for (int it = 0; it < 3; ++it) {
    const int n = (3 * w + it) * 64 + l;
    int so;
    if (jg) { const int k = n / 12; so = (64 * (jb >> 4) + k) * (DF * 2) + 192 * (jb & 15) + 16 * (n - 12 * k); }
    else { const int row = n / 21; so = n < 672 ? (8 * (jb & 7) + (row & 7) + 64 * (4 * ((jb >> 3) & 7) + (row >> 3))) * 642 + 306 * ((jb >> 6) & 1) + 16 * (n - 21 * row) : OOB; }
    DMA_M0(lds + (3 * w + it) * 1024);
    DMA16N(ra, so, 0, 0);
    DMA_M0(lds + 12288 + (3 * w + it) * 1024);
    DMA16N(rb, so, 0, 0);
  }
}
DI void retile_gu(int jb, rsrc_t rs, const u8* sd, int w, int l) {
  const int xx = l & 15, g = l >> 4;
  const u8* base = sd + ((l >> 1) & 1) * 12288 + (8 * g + (xx >> 2)) * 192 + 48 * w + 8 * (l & 1);
  const int vo = OFF_WGU + (((jb & 15) * 4 + w) * 20 + 2 * (jb >> 4)) * 3072 + (16 * g + 4 * ((xx & 7) >> 1) + 2 * (xx >> 3) + (xx & 1)) * 16;
#pragma unroll
  for (int i = 0; i < 6; ++i) {
    const u8* q = base + (i / 3) * 32 * 192 + (i % 3) * 16;
    const u2 lo = trread(q), hi = trread(q + 4 * 192);
    const u4 o = {lo[0], lo[1], hi[0], hi[1]};
    ST128C(o, rs, vo + i * 1024);
  }
}

#define W1A 8
#define W1B 18
#define W1C 26
#define W1D 36
constexpr int L_T1 = 0, L_MOD = 6144, L_PMX = 11264, L_HIMG = 11520, L_TIMG = 32768;
constexpr int L_SIDE = 57344;
constexpr int L_T32 = 86016, L_COL = 102400, L_EX = 118784;
constexpr int L_T4 = 0, L_MOD2 = 8192, L_US = 136192;

extern "C" __global__ __launch_bounds__(256, 1) void kmain(const u16* __restrict__ x, Wts p, u8* __restrict__ ws, u16* __restrict__ out) {
  __shared__ __attribute__((aligned(16))) u8 sm[163840];
  constexpr int US = 80;
  const int bid = blockIdx.x, bx = bid & 7, by = bid >> 3;
  const int P = 8 * (bx & 1) + (by >> 2), nb = 4 * (bx >> 1) + (by & 3);
  const int uu = (bx >> 1) & 1, db = 4 * (bx >> 2) + (by & 3);
  const int rb = 16 * P + nb;
  const int t = threadIdx.x, w = __builtin_amdgcn_readfirstlane(t >> 6), l = t & 63, xx = l & 15, g = l >> 4;
  const int l16 = l * 16;
  const u32 smb = LDSADDR(sm);
  const rsrc_t rt = mkrs(&g_tab, TEND * 1024);
  float ib;
  u4 wf[60];
  if (bid < 128) {
    if (w < 3) {
      DMA_M0(smb + L_T1 + w * 2048);
      DMA16(rt, l16, T1 * 1024 + w * 2048, 0);
      DMA16(rt, l16, T1 * 1024 + w * 2048, 1024);
    }
    u4 m2[5][2];
#pragma unroll
    for (int q = 0; q < 5; ++q) {
      m2[q][0] = LD128(rt, l16, (T2 + 10 * w + 2 * q) * 1024);
      m2[q][1] = LD128(rt, l16 + 1024, (T2 + 10 * w + 2 * q) * 1024);
    }
    SB();
    STAMP(1, 0);
    const rsrc_t rs = mkrs(ws, WS_END), rx = mkrs(x, S * D * 2);
    u4 mp0[4], mp1[4];
#pragma unroll
    for (int q = 0; q < 4; ++q) { mp0[q] = LD128(rs, t * 16, OFF_MODP + q * 15360); mp1[q] = LD128(rs, l < 16 ? l16 : OOB, OFF_MODP + q * 15360 + 4096 + w * 256); }
    u2 xa[4], xb[4]; u32 xc[4];
    const int xs = k1_row(bid, 4 * w) * 1280;
#pragma unroll
    for (int rr = 0; rr < 4; ++rr) {
      xa[rr] = LD64(rx, 8 * l, xs + rr * 81920); xb[rr] = LD64(rx, 8 * l + 512, xs + rr * 81920); xc[rr] = LD32(rx, 4 * l + 1024, xs + rr * 81920);
    }
    f4 st[2];
    st[0] = __builtin_bit_cast(f4, LD128(rs, 0, OFF_STAT + (bid * 16 + 4 * w) * 8));
    st[1] = __builtin_bit_cast(f4, LD128(rs, 16, OFF_STAT + (bid * 16 + 4 * w) * 8));
    SB();
    {
      const f4 a = (__builtin_bit_cast(f4, mp0[0]) + __builtin_bit_cast(f4, mp0[1])) + (__builtin_bit_cast(f4, mp0[2]) + __builtin_bit_cast(f4, mp0[3]));
      const f4 b = (__builtin_bit_cast(f4, mp1[0]) + __builtin_bit_cast(f4, mp1[1])) + (__builtin_bit_cast(f4, mp1[2]) + __builtin_bit_cast(f4, mp1[3]));
      const f4 a2 = rbf4(rbf4(a) + (t < 160 ? 0.f : 1.f)), b2 = rbf4(rbf4(b) + (l < 16 ? 1.f : 0.f));
      *(f4*)(sm + L_MOD + t * 16) = a2;
      if (l < 16) *(f4*)(sm + L_MOD + 4096 + w * 256 + l16) = b2;
      const u32 m = umax64(max(amax4(a2), amax4(b2)));
      if (l == 0) *(u32*)(sm + L_PMX + 4 * w) = m;
    }
    LGKMWAIT0();
    BAR();
    STAMP(1, 1);
    {
      const u4 pm = *(const u4*)(sm + L_PMX);
      const u8* mp = sm + L_MOD;
      const f4 s0 = *(const f4*)(mp + l16), s1 = *(const f4*)(mp + 1024 + l16), g0 = *(const f4*)(mp + 2560 + l16), g1 = *(const f4*)(mp + 3584 + l16);
      const f2 s2 = *(const f2*)(mp + 2048 + 8 * l), g2 = *(const f2*)(mp + 4608 + 8 * l);
      float ia_;
      const float alpha = pow2_norm(max(max(pm[0], pm[1]), max(pm[2], pm[3])), &ia_);
      if (bid == 0) ST128C(__builtin_bit_cast(u4, f4{ia_, ia_, ia_, ia_}), rs, t < 4 ? OFF_MODF + 10240 + l16 : OOB);
      const f4 sa0 = s0 * alpha, sa1 = s1 * alpha;
      const f2 sa2 = s2 * alpha;
#pragma unroll
      for (int rr = 0; rr < 4; ++rr) {
        const float r = st[rr >> 1][2 * (rr & 1) + 1] * alpha, mb = -st[rr >> 1][2 * (rr & 1)] * r;
        u8* hp = sm + L_HIMG + (4 * w + rr) * 1280;
        const f4 h0 = (unbf4(xa[rr]) * r + mb) * g0 + sa0, h1 = (unbf4(xb[rr]) * r + mb) * g1 + sa1;
        const f2 h2_ = (f2{bflo(xc[rr]), bfhi(xc[rr])} * r + mb) * g2 + sa2;
        *(u2*)(hp + 8 * l) = u2{pkh(h0[0], h0[1]), pkh(h0[2], h0[3])};
        *(u2*)(hp + 512 + 8 * l) = u2{pkh(h1[0], h1[1]), pkh(h1[2], h1[3])};
        *(u32*)(hp + 1024 + 4 * l) = pkh(h2_[0], h2_[1]);
      }
    }
    u4 m1[6];
#pragma unroll
    for (int q = 0; q < 6; ++q) m1[q] = *(const u4*)(sm + L_T1 + q * 1024 + l16);
    CBAR();
    {
      u4 a32[4], a32b[4];
#pragma unroll
      for (int rr = 0; rr < 4; ++rr) {
        const u8* hp = sm + L_HIMG + (4 * w + rr) * 1280;
        const u2 a0 = trread(hp + 8 * l), a1 = trread(hp + 512 + 8 * l), a2 = trread(hp + 1024 + 8 * (l & 31));
        a32[rr] = u4{a0[0], a0[1], a1[0], a1[1]}; a32b[rr] = u4{a2[0], a2[1], a2[0], a2[1]};
      }
      PIN0_4(a32[0], a32[1], a32[2], a32[3]);
      PIN2_4(a32b[0], a32b[1], a32b[2], a32b[3]);
      f4 ac[12];
#pragma unroll
      for (int i = 0; i < 12; ++i) { MH_NEW(ac[i], a32[i / 3], m1[2 * (i % 3)]); MH_ACC(ac[i], a32b[i / 3], m1[2 * (i % 3) + 1]); }
      PIN8_4(ac[8], ac[9], ac[10], ac[11]);
      PIN0_4(ac[0], ac[1], ac[2], ac[3]);
      PIN0_4(ac[4], ac[5], ac[6], ac[7]);
#pragma unroll
      for (int i = 0; i < 12; ++i) {
        u8* tp = sm + L_TIMG + (xx * 2 + (g >> 1)) * 256 + (((4 * w + i / 3) ^ (l & 7)) * 16) + (g & 1) * 8 + (i % 3) * 8192;
        if (i % 3 < 2 || xx < 8) *(u2*)tp = u2{pkh(ac[i][0], ac[i][1]), pkh(ac[i][2], ac[i][3])};
      }
    }
    LGKMWAIT0();
    BAR();
    STAMP(1, 2);
    {
      const int b = 4 * bx + (by & 3);
      const int vo = OFF_X1T + ((b >> 4) * 4 + (by >> 2)) * 1024 + ((b & 15) * 4 + g) * 16 + (80 * w + xx) * 8192;
      u4 av[5];
#pragma unroll
      for (int q = 0; q < 5; ++q) { const int sg = 5 * w + q; av[q] = *(const u4*)(sm + L_TIMG + (sg * 4 + g) * 256 + ((xx ^ ((2 * sg + (g >> 1)) & 7)) * 16)); }
      f4 re[5], im[5];
#pragma unroll
      for (int q = 0; q < 5; ++q) { MH_NEW(re[q], av[q], m2[q][0]); MH_NEW(im[q], av[q], m2[q][1]); }
      PIN8_4(re[3], im[3], re[4], im[4]);
      PIN0_4(re[0], im[0], re[1], im[1]);
      PIN0_2(re[2], im[2]);
#pragma unroll
      for (int q = 0; q < 5; ++q) {
        const u4 o = {pkh(re[q][0], im[q][0]), pkh(re[q][1], im[q][1]), pkh(re[q][2], im[q][2]), pkh(re[q][3], im[q][3])};
        ST128C(o, rs, vo + q * 131072);
      }
    }
    STAMP(1, 3);
  } else {
    const u4 ctab = LD128(rt, xx * 16, TCOL * 1024 + ((bid >> 6) & 1) * 512 + (w >> 1) * 256);
    SB();
    STAMP(1, 0);
    const rsrc_t rs = mkrs(ws, WS_END);
    stage_pair(false, bid, p, smb, w, l);
    stage_pair(true, bid - 128, p, smb + 24576, w, l);
    u4 mq[4];
    const int mo_ = (w == 3 && l < 8 && bid < 208) ? (bid - 128) * 128 + l16 : OOB;
#pragma unroll
    for (int q = 0; q < 4; ++q) mq[q] = LD128(rs, mo_, OFF_MODP + q * 15360 + 5120);
    SB();
    VMWAIT(10);
    BAR();
    STAMP(1, 1);
    {
      constexpr int RS = 336;
      typedef u16 us2 __attribute__((ext_vector_type(2)));
      const int a = bid & 7, q = (bid >> 3) & 7, hf = (bid >> 6) & 1, pp = w & 1;
      const u8* base = sm + ((xx >> 2) & 1) * 12288 + ((xx >> 3) * 8 + 2 * g + pp) * RS + 32 * (w >> 1) + 16 * hf + 8 * (l & 3);
      const int vo = OFF_WT + (((2 * (a >> 2) + pp) * 2 + (q >> 2)) * 64 + (q & 3) * 16 + 4 * (a & 3) + g) * 16;
      u32 mx = 0;
#pragma unroll
      for (int it = 0; it < 6; ++it) {
        const u2 lo = trread(base + 64 * it), hi = trread(base + 64 * it + 16 * RS);
        const u32 c = (it & 1) ? ctab[it >> 1] >> 16 : ctab[it >> 1] & 0xffffu;
        const u4 o = {lo[0], lo[1], hi[0], hi[1]};
        ST128C(o, rs, c == 0xffffu ? OOB : vo + (int)c * 8192);
#pragma unroll
        for (int e = 0; e < 4; ++e) mx = __builtin_bit_cast(u32, __builtin_elementwise_max(__builtin_bit_cast(us2, mx), __builtin_bit_cast(us2, o[e] & 0x7fff7fffu)));
      }
      mx = umax64(max(mx << 16, mx & 0xffff0000u));
      __builtin_amdgcn_raw_buffer_store_b32(mx, rs, l == 0 ? 4 * w : OOB, OFF_WMAX + (bid - 128) * 16, 16);
    }
    {
      const f4 a = (__builtin_bit_cast(f4, mq[0]) + __builtin_bit_cast(f4, mq[1])) + (__builtin_bit_cast(f4, mq[2]) + __builtin_bit_cast(f4, mq[3]));
      ST128C(__builtin_bit_cast(u4, rbf4(rbf4(a) + ((bid >= 168 && bid < 188) ? 1.f : 0.f))), rs, mo_ + OFF_MODF);
    }
    BAR();
    STAMP(1, 2);
    retile_gu(bid - 128, rs, sm + 24576, w, l);
    STAMP(1, 3);
  }
  const rsrc_t rs = mkrs(ws, WS_END), rx = mkrs(x, S * D * 2);
  gu32* cnt = (gu32*)(ws + OFF_CNT);
  VMWAIT(0);
  BAR();
  EDGE_SIGNAL(cnt + (by & 15) * 64);
  STAMP(1, 4);
  u4 m3[5][2];
  const int dlo = 32 * xx + 8 * g, dl2 = xx < 8 ? dlo : OOB;
  u2 xv[2][3];
  float iav;
  const int w1s = OFF_WGU + (nb * 4 + w) * 61440;
#define W1LOAD(A, B) do { SB(); sfor<A, B>(LAM(I_) { constexpr int i_ = decltype(I_)::v; \
    wf[i_] = LD128C(rs, l16 + (i_ & 3) * 1024, w1s + (i_ >> 2) * 4096); }); SB(); } while (0)
  {
    const int tn = w & 1, tt = w >> 1;
    DMA_M0(smb + L_T32 + w * 4096);
    DMA16(rt, l16, T32 * 1024 + w * 4096, 0);
    DMA16(rt, l16, T32 * 1024 + w * 4096, 1024);
    DMA16(rt, l16, T32 * 1024 + w * 4096, 2048);
    DMA16(rt, l16, T32 * 1024 + w * 4096, 3072);
    if (bid >= 160 && w > 0) {
      const rsrc_t rd = mkrs(p.wd, DF * D * 2);
#pragma unroll
      for (int i = 0; i < 7; ++i) {
        const int pc = 7 * (w - 1) + i, n = pc * 64 + l, row = n / 40;
        DMA_M0(smb + L_SIDE + pc * 1024);
        DMA16N(rd, pc < 20 ? row * 1280 + (n - 40 * row) * 16 : OOB, ((bid - 160) >> 1) * 40960 + (bid & 1) * 640, 0);
      }
    }
    if (bid >= 128 && bid < 160 && w > 0) {
      stage_pair(true, bid, p, smb + L_SIDE, w, l);
      if (w == 1) stage_pair(true, bid, p, smb + L_SIDE, 0, l);
    }
    SB();
    f4 c1[2], s1[2], c2[2], s2[2];
#pragma unroll
    for (int h = 0; h < 2; ++h)
#pragma unroll
      for (int r = 0; r < 4; ++r) {
        const float a1 = (float)((16 * tn + 4 * g + r) * (32 * h + 16 * tt + xx)) * (1.f / 2048.f);
        c1[h][r] = __builtin_amdgcn_cosf(a1); s1[h][r] = __builtin_amdgcn_sinf(a1);
        const float a2 = (float)((32 * tt + 8 * g + 2 * r + tn) * (16 * h + xx)) * (1.f / 2048.f);
        c2[h][r] = __builtin_amdgcn_cosf(a2); s2[h][r] = __builtin_amdgcn_sinf(a2);
      }
    PIN0_4(c1[0], c1[1], s1[0], s1[1]);
    PIN0_4(c2[0], c2[1], s2[0], s2[1]);
    if (w == 0) wait_edge(cnt + (bx * 16 + xx) * 64);
    BAR();
    STAMP(2, 0);
    const bool two = bid < 65;
    const int cB = bid == 0 ? 320 : 255 + bid, cinB = bid == 0 ? 0 : cB;
    DMA_M0(smb + L_COL + w * 2048);
    DMA16C(rs, l16, OFF_X1T + bid * 8192 + w * 2048, 0);
    DMA16C(rs, l16, OFF_X1T + bid * 8192 + w * 2048, 1024);
    DMA_M0(smb + L_COL + 8192 + w * 2048);
    DMA16C(rs, two ? l16 : OOB, OFF_X1T + cinB * 8192 + w * 2048, 0);
    DMA16C(rs, two ? l16 : OOB, OFF_X1T + cinB * 8192 + w * 2048, 1024);
    u4 wm[2], wt[2][2];
    iav = __builtin_bit_cast(float, LD32C(rs, 0, OFF_MODF + 10240));
    wm[0] = LD128C(rs, l16, OFF_WMAX); wm[1] = LD128C(rs, l16 + 1024, OFF_WMAX);
    wt[0][0] = LD128C(rs, l16, OFF_WT + bid * 8192 + w * 2048); wt[0][1] = LD128C(rs, l16 + 1024, OFF_WT + bid * 8192 + w * 2048);
    wt[1][0] = LD128C(rs, two ? l16 : OOB, OFF_WT + cB * 8192 + w * 2048); wt[1][1] = LD128C(rs, two ? l16 + 1024 : OOB, OFF_WT + cB * 8192 + w * 2048);
#pragma unroll
    for (int q = 0; q < 5; ++q) {
      m3[q][0] = LD128(rt, l16, (T3 + 10 * w + 2 * q) * 1024);
      m3[q][1] = LD128(rt, l16 + 1024, (T3 + 10 * w + 2 * q) * 1024);
    }
    SB();
    VMWAIT(17);
    BAR();
    STAMP(2, 1);
    u4 me[2][2], mo[2][2], ma[2][2][2];
#pragma unroll
    for (int q = 0; q < 4; ++q) { me[q >> 1][q & 1] = *(const u4*)(sm + L_T32 + (tt * 4 + q) * 1024 + l16); mo[q >> 1][q & 1] = *(const u4*)(sm + L_T32 + 8192 + (tt * 4 + q) * 1024 + l16); }
#pragma unroll
    for (int q = 0; q < 8; ++q) ma[q >> 2][(q >> 1) & 1][q & 1] = *(const u4*)(sm + L_T32 + q * 1024 + l16);
    auto colfft = [&](auto NC_) __attribute__((always_inline)) {
      constexpr int NC = decltype(NC_)::v;
      u4 av[NC][4];
      f4 er[NC], ei[NC], orr[NC], oi[NC];
      float beta;
#define OUTER_STAGE() _Pragma("unroll") for (int u = 0; u < NC; ++u) { \
        MH_NEW(er[u], av[u][0], me[0][0]); MH_NEW(ei[u], av[u][0], me[1][0]); MH_NEW(orr[u], av[u][2], mo[0][0]); MH_NEW(oi[u], av[u][2], mo[1][0]); \
        MH_ACC(er[u], av[u][1], me[0][1]); MH_ACC(ei[u], av[u][1], me[1][1]); MH_ACC(orr[u], av[u][3], mo[0][1]); MH_ACC(oi[u], av[u][3], mo[1][1]); }
#pragma unroll
      for (int u = 0; u < NC; ++u)
#pragma unroll
        for (int q = 0; q < 4; ++q) av[u][q] = *(const u4*)(sm + L_COL + u * 8192 + (tn * 4 + q) * 1024 + (xx * 4 + g) * 16);
      if (NC == 2 && bid == 0) {
#pragma unroll
        for (int q = 0; q < 4; ++q) { av[0][q] &= 0xffffu; av[NC - 1][q] >>= 16; }
      }
#pragma unroll
      for (int u = 0; u < NC; ++u) PIN2_4(av[u][0], av[u][1], av[u][2], av[u][3]);
      OUTER_STAGE();
      beta = pow2_norm(umax64(max(max(max(wm[0][0], wm[0][1]), max(wm[0][2], wm[0][3])), max(max(wm[1][0], wm[1][1]), max(wm[1][2], wm[1][3])))), &ib);
      PIN8_4(er[NC - 1], ei[NC - 1], orr[NC - 1], oi[NC - 1]);
      PIN0_4(er[0], ei[0], orr[0], oi[0]);
#pragma unroll
      for (int u = 0; u < NC; ++u) {
        u8* e1 = sm + L_EX + u * 8192 + ((xx & 1) * 2 + tn) * 1024 + (g * 16 + ((8 * tt + (xx >> 1)) ^ (4 * (xx & 1)))) * 16;
        *(u4*)e1 = cmul_pack(er[u] + orr[u], ei[u] + oi[u], c1[0], s1[0]);
        *(u4*)(e1 + 4096) = cmul_pack(er[u] - orr[u], ei[u] - oi[u], c1[1], s1[1]);
      }
      DMA_M0(smb + L_T4 + w * 2048);
      DMA16(rt, l16, T4 * 1024 + w * 2048, 0);
      DMA16(rt, l16, T4 * 1024 + w * 2048, 1024);
      DMA_M0(smb + L_MOD2 + w * 2048);
      DMA16C(rs, l16, OFF_MODF + w * 2048, 0);
      DMA16C(rs, l16, OFF_MODF + w * 2048, 1024);
#pragma unroll
      for (int tn_ = 0; tn_ < 3; ++tn_) {
        xv[0][tn_] = LD64(rx, (tn_ == 2 ? dl2 : dlo) + tn_ * 512, (8 * rb + 2 * w) * 1280);
        xv[1][tn_] = LD64(rx, (tn_ == 2 ? dl2 : dlo) + tn_ * 512 + 1280, (8 * rb + 2 * w) * 1280);
      }
      SB();
      LGKMWAIT0();
      BAR();
      {
        u4 b0[NC], b1[NC], yv[NC][2];
        f4 re[NC][2], im[NC][2];
#pragma unroll
        for (int u = 0; u < NC; ++u) {
          const u8* e1 = sm + L_EX + u * 8192 + w * 2048 + (g * 16 + (xx ^ (4 * tn))) * 16;
          b0[u] = *(const u4*)e1; b1[u] = *(const u4*)(e1 + 1024);
        }
#pragma unroll
        for (int u = 0; u < NC; ++u) {
          MH_NEW(re[u][0], ma[0][0][0], b0[u]); MH_NEW(im[u][0], ma[0][1][0], b0[u]); MH_NEW(re[u][1], ma[1][0][0], b0[u]); MH_NEW(im[u][1], ma[1][1][0], b0[u]);
          MH_ACC(re[u][0], ma[0][0][1], b1[u]); MH_ACC(im[u][0], ma[0][1][1], b1[u]); MH_ACC(re[u][1], ma[1][0][1], b1[u]); MH_ACC(im[u][1], ma[1][1][1], b1[u]);
        }
        f4 wr[NC][2], wi[NC][2];
#pragma unroll
        for (int u = 0; u < NC; ++u)
#pragma unroll
          for (int h = 0; h < 2; ++h) {
            wr[u][h] = f4{bflo(wt[u][h][0]), bflo(wt[u][h][1]), bflo(wt[u][h][2]), bflo(wt[u][h][3])} * beta;
            wi[u][h] = f4{bfhi(wt[u][h][0]), bfhi(wt[u][h][1]), bfhi(wt[u][h][2]), bfhi(wt[u][h][3])} * beta;
          }
        PIN8_4(re[NC - 1][0], im[NC - 1][0], re[NC - 1][1], im[NC - 1][1]);
        PIN0_4(re[0][0], im[0][0], re[0][1], im[0][1]);
#pragma unroll
        for (int u = 0; u < NC; ++u) {
#pragma unroll
          for (int h = 0; h < 2; ++h) {
            const f4 a = re[u][h] * wr[u][h] - im[u][h] * wi[u][h], b = -(re[u][h] * wi[u][h]) - im[u][h] * wr[u][h];
            yv[u][h] = u4{pkh(a[0], b[0]), pkh(a[1], b[1]), pkh(a[2], b[2]), pkh(a[3], b[3])};
          }
          PIN2_2(yv[u][0], yv[u][1]);
          MH_NEW(re[u][0], yv[u][0], ma[0][0][0]); MH_NEW(im[u][0], yv[u][0], ma[0][1][0]); MH_NEW(re[u][1], yv[u][0], ma[1][0][0]); MH_NEW(im[u][1], yv[u][0], ma[1][1][0]);
          MH_ACC(re[u][0], yv[u][1], ma[0][0][1]); MH_ACC(im[u][0], yv[u][1], ma[0][1][1]); MH_ACC(re[u][1], yv[u][1], ma[1][0][1]); MH_ACC(im[u][1], yv[u][1], ma[1][1][1]);
        }
        PIN8_4(re[NC - 1][0], im[NC - 1][0], re[NC - 1][1], im[NC - 1][1]);
        PIN0_4(re[0][0], im[0][0], re[0][1], im[0][1]);
#pragma unroll
        for (int u = 0; u < NC; ++u)
#pragma unroll
          for (int h = 0; h < 2; ++h) *(u4*)(sm + L_COL + u * 8192 + (h * 4 + 2 * tn + tt) * 1024 + l16) = cmul_pack(re[u][h], im[u][h], c2[h], s2[h]);
      }
      W1LOAD(0, W1A);
      LGKMWAIT0();
      BAR();
#pragma unroll
      for (int u = 0; u < NC; ++u)
#pragma unroll
        for (int q = 0; q < 4; ++q) av[u][q] = *(const u4*)(sm + L_COL + u * 8192 + (tn * 4 + q) * 1024 + l16);
      OUTER_STAGE();
      PIN8_4(er[NC - 1], ei[NC - 1], orr[NC - 1], oi[NC - 1]);
      PIN0_4(er[0], ei[0], orr[0], oi[0]);
      if (NC == 2 && bid == 0) { ei[0] = er[NC - 1]; oi[0] = orr[NC - 1]; }
#pragma unroll
      for (int u = 0; u < NC; ++u) {
        const int ci = u ? cB : bid;
        const int vo = (u && bid == 0) ? OOB : OFF_X2T + ((ci >> 2) * 8 + tt * 2 + tn) * 4096 + (ci & 3) * 1024 + (xx * 4 + g) * 16;
#pragma unroll
        for (int h = 0; h < 2; ++h) {
          const f4 re = h ? er[u] - orr[u] : er[u] + orr[u], im = h ? ei[u] - oi[u] : ei[u] + oi[u];
          const u4 o = {pkh(re[0], im[0]), pkh(re[1], im[1]), pkh(re[2], im[2]), pkh(re[3], im[3])};
          ST128C(o, rs, vo + h * 16384);
        }
      }
    };
    if (two) colfft(IC<2>{}); else colfft(IC<1>{});
    STAMP(2, 3);
    if (bid >= 128 && bid < 160) retile_gu(bid, rs, sm + L_SIDE, w, l);
    if (bid >= 160) {
      const u8* base = sm + L_SIDE + (8 * g + (xx >> 2)) * 640 + 160 * w + 8 * (l & 3);
      const int kap = (bid - 160) >> 1;
      const int vo = OFF_WD + (((4 * (bid & 1) + w) * 4 + kap / 12) * 12 + kap % 12) * 5120 + l16;
#pragma unroll
      for (int it = 0; it < 5; ++it) {
        const u2 lo = trread(base + 32 * it), hi = trread(base + 32 * it + 2560);
        const u4 o = {lo[0], lo[1], hi[0], hi[1]};
        ST128C(o, rs, vo + it * 1024);
      }
    }
  }
  VMWAIT(0);
  BAR();
  EDGE_SIGNAL(cnt + (128 + (by & 15)) * 64);
  STAMP(2, 4);
  {
    u8* us = sm + L_US;
    W1LOAD(W1A, W1B);
    if (w == 0) wait_edge(cnt + (128 + bx * 16 + xx) * 64);
    BAR();
    STAMP(3, 0);
    const int n1r = rb >> 2;
    const int gvo = g * 32768 + 4 * (l & 7);
    const int gso = OFF_X2T + ((n1r >> 4) * 2 + ((rb >> 1) & 1)) * 4096 + ((n1r & 15) * 4 + 2 * (rb & 1)) * 16 + 5 * w * 131072;
    u4 bv[5];
    sfor<0, 5>(LAM(Q_) {
      constexpr int q = decltype(Q_)::v;
      sfor<0, 4>(LAM(J_) { constexpr int j = decltype(J_)::v; bv[q][j] = LD32C(rs, gvo + j * 1024, gso + q * 131072); });
    });
    W1LOAD(W1B, W1C);
    f4 re[5], im[5];
#pragma unroll
    for (int q = 0; q < 5; ++q) { MH_NEW(re[q], m3[q][0], bv[q]); MH_NEW(im[q], m3[q][1], bv[q]); }
    asm volatile("s_nop 7" : "+v"(re[0]), "+v"(im[0]), "+v"(re[1]), "+v"(im[1]), "+v"(re[2]), "+v"(im[2]), "+v"(re[3]), "+v"(im[3]), "+v"(re[4]), "+v"(im[4]));
    {
      u8* p = us + ((xx & 7) * 16 + 4 * g) * US;
#pragma unroll
      for (int r = 0; r < 4; ++r) {
        const u4 o = {pkh(re[0][r], im[0][r]), pkh(re[1][r], im[1][r]), pkh(re[2][r], im[2][r]), pkh(re[3][r], im[3][r])};
        *(u4*)(p + r * US + 16 * w) = o;
        *(u32*)(p + r * US + 64 + 4 * w) = pkh(re[4][r], im[4][r]);
      }
    }
    LGKMWAIT0();
    BAR();
    STAMP(3, 1);
    W1LOAD(W1C, W1D);
    f4 y[2][3];
    {
      const u8* p = us + (2 * w * 16 + xx) * US;
      u4 a32[2], a32b[2], m4[3], m4b[3];
#pragma unroll
      for (int tn = 0; tn < 3; ++tn) { m4[tn] = *(const u4*)(sm + L_T4 + tn * 2048 + l16); m4b[tn] = *(const u4*)(sm + L_T4 + tn * 2048 + 1024 + l16); }
#pragma unroll
      for (int rr = 0; rr < 2; ++rr) {
        a32[rr] = *(const u4*)(p + rr * 16 * US + 16 * g);
        const u2 a16 = *(const u2*)(p + rr * 16 * US + 64 + 8 * (g & 1));
        a32b[rr] = u4{a16[0], a16[1], a16[0], a16[1]};
      }
      asm volatile("s_nop 1" : "+v"(a32b[0]), "+v"(a32b[1]));
#pragma unroll
      for (int i = 0; i < 6; ++i) MH_NEW(y[i / 3][i % 3], a32[i / 3], m4[i % 3]);
#pragma unroll
      for (int i = 0; i < 6; ++i) MH_ACC(y[i / 3][i % 3], a32b[i / 3], m4b[i % 3]);
    }
    const float sc = (0.4f * iav) * ib;
    f4 gaf[3], cbf[3], sbf[3];
#pragma unroll
    for (int tn = 0; tn < 3; ++tn) {
      const u8* mp = sm + L_MOD2 + 2 * dlo + tn * 1024;
      gaf[tn] = *(const f4*)mp * sc; sbf[tn] = *(const f4*)(mp + 2560); cbf[tn] = *(const f4*)(mp + 5120);
    }
    asm volatile("s_nop 7" : "+v"(y[0][0]), "+v"(y[0][1]), "+v"(y[0][2]), "+v"(y[1][0]), "+v"(y[1][1]), "+v"(y[1][2]));
    const int x1s = OFF_X1R + (8 * rb + 2 * w) * 1280;
    float mean[2], rsd[2];
#pragma unroll
    for (int rr = 0; rr < 2; ++rr) {
#pragma unroll
      for (int tn = 0; tn < 3; ++tn) {
        const u2 xq = xv[rr][tn];
        y[rr][tn] = y[rr][tn] * gaf[tn] + f4{bflo(xq[0]), bfhi(xq[0]), bflo(xq[1]), bfhi(xq[1])};
        const u2 o1 = {pkbf(y[rr][tn][0], y[rr][tn][1]), pkbf(y[rr][tn][2], y[rr][tn][3])};
        __builtin_amdgcn_raw_buffer_store_b64(o1, rs, (tn == 2 ? dl2 : dlo) + tn * 512 + rr * 1280, x1s, 16);
      }
      const f4 s4_ = (y[rr][0] + y[rr][1]) + y[rr][2];
      mean[rr] = (s4_[0] + s4_[1]) + (s4_[2] + s4_[3]);
    }
    mean[0] = sum64(mean[0]) * (1.f / 640.f);
    mean[1] = sum64(mean[1]) * (1.f / 640.f);
#pragma unroll
    for (int rr = 0; rr < 2; ++rr) {
      y[rr][0] -= mean[rr];
      y[rr][1] -= mean[rr];
      y[rr][2] -= xx < 8 ? mean[rr] : 0.f;
      const f4 q4 = y[rr][0] * y[rr][0] + (y[rr][1] * y[rr][1] + y[rr][2] * y[rr][2]);
      rsd[rr] = (q4[0] + q4[1]) + (q4[2] + q4[3]);
    }
    rsd[0] = __builtin_amdgcn_rsqf(sum64(rsd[0]) * (1.f / 640.f) + 1e-5f);
    rsd[1] = __builtin_amdgcn_rsqf(sum64(rsd[1]) * (1.f / 640.f) + 1e-5f);
    const int h2s = OFF_H2 + (rb >> 2) * 40960 + (rb & 3) * 1024 + 2 * w * 128;
#pragma unroll
    for (int rr = 0; rr < 2; ++rr) {
      const int ho = (xx >> 2) * 4096 + rr * 128 + (((2 * (xx & 3) + (g >> 1)) ^ (2 * w + rr)) * 16) + 8 * (g & 1);
      const int ho2 = xx < 8 ? ho : OOB;
#pragma unroll
      for (int tn = 0; tn < 3; ++tn) {
        const f4 hh = (y[rr][tn] * rsd[rr]) * cbf[tn] + sbf[tn];
        const u2 o2 = {pkbf(hh[0], hh[1]), pkbf(hh[2], hh[3])};
        __builtin_amdgcn_raw_buffer_store_b64(o2, rs, tn == 2 ? ho2 : ho, h2s + tn * 16384, 16);
      }
    }
  }
  STAMP(3, 2);
  VMWAIT(0);
  BAR();
  ATOMIC_INC(0, cnt + (256 + P) * 64);
  STAMP(3, 3);
  W1LOAD(W1D, 60);
  if (w == 0) wait_cnt(cnt + (256 + P) * 64, 16u);
  VMWAIT(0);
  BAR();
  STAMP(3, 4);
  const u32 dvo = OFF_H2 + (u32)(P * 163840 + w * 10240 + l * 16);
  const u32 smw = smb + w * 10240;
#define DMA_PIECE(G, I) do { if constexpr ((I) % 4 == 0) DMA_M0(smw + (G) * 40960 + (I) * 1024); DMA16C(rs, dvo, (G) * 40960 + ((I) & ~3) * 1024, ((I) & 3) * 1024); } while (0)
  sfor<0, 20>(LAM(I_) { constexpr int i = decltype(I_)::v; DMA_PIECE(i / 10, i % 10); });
  VMWAIT(10);
  BAR();
  STAMP(3, 5);
  STAMPC(0);
  u32 tb[2][2];
  tb[0][0] = LDSADDR(sm) + ((l >> 3) & 1) * 1024 + (l & 7) * 128 + ((g ^ (l & 7)) * 16);
  tb[0][1] = tb[0][0] ^ 64u;
  tb[1][0] = tb[0][0] + 81920u;
  tb[1][1] = tb[0][1] + 81920u;
  asm volatile("" : "+v"(tb[0][0]), "+v"(tb[0][1]), "+v"(tb[1][0]), "+v"(tb[1][1]));
  u32 tc[2][2];
  tc[0][0] = tb[0][0] + 40960u; tc[0][1] = tb[0][1] + 40960u; tc[1][0] = tb[1][0] + 40960u; tc[1][1] = tb[1][1] + 40960u;
  asm volatile("" : "+v"(tc[0][0]), "+v"(tc[0][1]), "+v"(tc[1][0]), "+v"(tc[1][1]));
  const int w2s = OFF_WD + (db * 4 + w) * 61440;
  int hoff[3];
#pragma unroll
  for (int tau = 0; tau < 3; ++tau) { const int o = 3 * w + tau; hoff[tau] = OFF_H + (P * 48 + 3 * nb + (o >> 2)) * 8192 + (o & 3) * 256 + xx * 16 + 4 * g; }
  u4 w2[60];
  f4 acc[2][2][3];
  u4 tf[3][2];
  u4 hf[4][12];
  float e0, e1, q0, q1;
  u32 hv = 0;
  const int hbase = OFF_H + (P * 48 + 12 * w) * 8192 + uu * 1024 + l * 16;
  gu32* cntB = cnt + (272 + P * 4) * 64;
  u32 pv = 0, pv1 = 0;
  QDECL;
#define TADDR(S) (((((S) / 20) & 1) ? tc : tb)[(S) / 40][(S) & 1] + (((S) % 20) >> 1) * 4096)
#define TREAD(S) do { tf[(S) % 3][0] = *(const LAS u4*)(size_t)(TADDR(S)); tf[(S) % 3][1] = *(const LAS u4*)(size_t)(TADDR(S) + 2048); } while (0)
#define ACC_T(GP, IT) acc[(GP) & 1][(IT) / 3][(IT) % 3]
#define HSTORE(GP, IT) __builtin_amdgcn_raw_buffer_store_b32(hv, rs, hoff[(IT) % 3], (2 * (GP) + (IT) / 3) * 1024, 16)
#define EPI(GP, E) do { constexpr int it_ = (E) < 66 ? (E) / 11 : 5, op_ = (E) < 66 ? (E) % 11 : 11; const f4 a_ = ACC_T(GP, it_); \
    if constexpr (op_ == 0) { if constexpr (it_ == 0) e0 = a_[0] * a_[0]; else HSTORE(GP, it_ - 1); e1 = a_[1] * a_[1]; } \
    else if constexpr (op_ == 1) { e0 = __builtin_fmaf(e0, -0.1029432397f, -2.3022081986f); e1 = __builtin_fmaf(e1, -0.1029432397f, -2.3022081986f); } \
    else if constexpr (op_ == 2) { e0 *= a_[0]; e1 *= a_[1]; } \
    else if constexpr (op_ == 3) { e0 = __builtin_amdgcn_exp2f(e0); } \
    else if constexpr (op_ == 4) { e1 = __builtin_amdgcn_exp2f(e1); } \
    else if constexpr (op_ == 5) { e0 += 1.f; e1 += 1.f; } \
    else if constexpr (op_ == 6) { e0 = __builtin_amdgcn_rcpf(e0); } \
    else if constexpr (op_ == 7) { e1 = __builtin_amdgcn_rcpf(e1); } \
    else if constexpr (op_ == 8) { q0 = a_[0] * a_[2]; q1 = a_[1] * a_[3]; } \
    else if constexpr (op_ == 9) { q0 *= e0; q1 *= e1; } \
    else if constexpr (op_ == 10) { hv = pkbf(q0, q1); asm volatile("" : "+v"(hv)); \
      if constexpr (it_ < 5) { const float n_ = ACC_T(GP, it_ < 5 ? it_ + 1 : 5)[0]; e0 = n_ * n_; } } \
    else { HSTORE(GP, 5); } \
    if constexpr (op_ < 8 || (op_ == 10 && it_ < 5)) asm volatile("" : "+v"(e0)); \
    if constexpr (op_ < 8) asm volatile("" : "+v"(e1)); \
    if constexpr (op_ == 8 || op_ == 9) { asm volatile("" : "+v"(q0)); asm volatile("" : "+v"(q1)); } } while (0)
#define HLOAD(T, KS) hf[T][KS] = __builtin_amdgcn_raw_buffer_load_b128(rs, hbase, (KS) * 8192 + (T) * 2048, 16)
#define W2LOAD(I) w2[I] = __builtin_amdgcn_raw_buffer_load_b128(rs, l16 + ((I) & 3) * 1024, w2s + ((I) >> 2) * 4096, 16)
#define POLL_ISSUE(T) pv = __hip_atomic_load(cntB + (T) * 64, RLX_AGENT)
#define POLL_CHECK(T) do { if (__builtin_amdgcn_readfirstlane(pv) < 16u) wait_cnt(cntB + (T) * 64, 16u); \
    __builtin_amdgcn_fence(__ATOMIC_ACQUIRE, "wavefront"); SB(); } while (0)
#define PUBLISH(T) do { BAR(); ATOMIC_INC(T, cntB + (T) * 64); SB(); } while (0)
  e0 = e1 = q0 = q1 = 0.f;
  TREAD(0);
  TREAD(1);
  sfor<0, 4>(LAM(G_) {
    sfor<0, 20>(LAM(K_) {
      constexpr int G = decltype(G_)::v, ks = decltype(K_)::v, st = 20 * G + ks;
      if constexpr (st == 17) { VMWAIT(17); BAR(); SB(); }
      if constexpr (st == 27) { VMWAIT(vm_between(6 * 9, 6 * st)); BAR(); SB(); STAMPC(1); }
      if constexpr (st == PUB0) { VMWAIT(vm_between(last_store(0), 6 * st)); PUBLISH(0); STAMPC(2); }
      if constexpr (st == PUB1) { VMWAIT(vm_between(last_store(1), 6 * st)); PUBLISH(1); STAMPC(3); }
      if constexpr (st == PC0) POLL_CHECK(0);
      sfor<0, 6>(LAM(J_) {
        constexpr int j = decltype(J_)::v, tt = j / 3, tau = j % 3;
        if constexpr (ks == 0) MF_NEW(acc[G & 1][tt][tau], "a", wf[ks * 3 + tau], "v", tf[st % 3][tt]);
        else MF_ACC(acc[G & 1][tt][tau], "a", wf[ks * 3 + tau], "v", tf[st % 3][tt]);
        SB();
        if constexpr (j == 5 && st + 1 < 80) { __builtin_amdgcn_s_waitcnt(st + 2 < 80 ? 0xC27F : 0xC07F); SB(); }
        if constexpr (j == 3 && st + 2 < 80) TREAD(st + 2);
        if constexpr (j == 0) {
          if constexpr (G == 0) DMA_PIECE(2 + ks / 10, ks % 10);
          if constexpr (g1_w2(st, 0) >= 0) W2LOAD(g1_w2(st, 0));
          if constexpr (g1_w2(st, 1) >= 0) W2LOAD(g1_w2(st, 1));
          if constexpr (hsel(st, PC0, 0) >= 0) HLOAD(0, hsel(st, PC0, 0));
          if constexpr (hsel(st, PC0, 1) >= 0) HLOAD(0, hsel(st, PC0, 1));
        }
        if constexpr (j == 1 && st == PI0) POLL_ISSUE(0);
        if constexpr (j == 1 && st == PI1) { pv1 = __hip_atomic_load(cntB + 64, RLX_AGENT); }
        if constexpr (g1_epi(st, j) >= 0) EPI(G - 1, g1_epi(st, j));
        SB();
      });
    });
  });
  STAMP(3, 6);
  STAMPC(4);
  f4* red = (f4*)sm;
  f4 a2[2][5];
  const int tok = 128 * P + 32 * w + 16 * uu + xx;
  int x1o = OFF_X1R + (tok * D + 80 * db + 4 * g) * 2;
  asm volatile("" : "+v"(x1o));
  u2 xw[5];
  f4 gv[5];
  sfor<0, 4>(LAM(P_) {
    constexpr int ph = decltype(P_)::v;
    sfor<0, 12>(LAM(K_) {
      constexpr int ks = decltype(K_)::v, k = 12 * ph + ks;
      QSTAMP(k);
      if constexpr (k == PC1) { pv = pv1; POLL_CHECK(1); }
      if constexpr (k == PUB2) { VMWAIT(vm_between(last_store(2), 480 + 5 * k)); PUBLISH(2); }
      if constexpr (k == PC2) POLL_CHECK(2);
      if constexpr (k == PUB3) { VMWAIT(vm_between(last_store(3), 480 + 5 * k)); PUBLISH(3); }
      if constexpr (k == PW3) {
        wait_cnt(cntB + 192, 16u);
        sfor<0, 12>(LAM(Q_) { HLOAD(3, decltype(Q_)::v); });
        SB();
      }
      sfor<0, 5>(LAM(T_) {
        constexpr int tau = decltype(T_)::v, i = ks * 5 + tau, n = 5 * k + tau;
        if constexpr (ks == 0) MF_NEW(a2[ph & 1][tau], "v", w2[i], "a", hf[ph][ks]);
        else if constexpr (i < W2V) MF_ACC(a2[ph & 1][tau], "v", w2[i], "a", hf[ph][ks]);
        else MF_ACC(a2[ph & 1][tau], "a", w2[i], "a", hf[ph][ks]);
        SB();
        if constexpr (g2_epi(n, 0) >= 0) EPI(3, g2_epi(n, 0));
        if constexpr (g2_epi(n, 1) >= 0) EPI(3, g2_epi(n, 1));
        if constexpr (tau == 1) {
          if constexpr (hsel(k, PC1, 0) >= 0) HLOAD(1, hsel(k, PC1, 0));
          if constexpr (hsel(k, PC1, 1) >= 0) HLOAD(1, hsel(k, PC1, 1));
          if constexpr (hsel(k, PC2, 0) >= 0) HLOAD(2, hsel(k, PC2, 0));
          if constexpr (hsel(k, PC2, 1) >= 0) HLOAD(2, hsel(k, PC2, 1));
        }
        if constexpr (tau == 3 && k == PI2) POLL_ISSUE(2);
        if constexpr (tau == 3 && k >= 36 && k < 41) xw[k - 36] = __builtin_amdgcn_raw_buffer_load_b64(rs, x1o + 32 * (k - 36), 0, 16);
        if constexpr (tau == 1 && k >= 36 && k < 41) gv[k - 36] = __builtin_bit_cast(f4, LD128C(rs, 16 * g + 64 * (k - 36), OFF_MODF + (3 * 640 + 80 * db) * 4));
        if constexpr (ph > 0 && tau == 2 && ks >= 3 && ks < 8) red[(w * 20 + (ph - 1) * 5 + ks - 3) * 64 + l] = a2[(ph - 1) & 1][ks - 3];
        SB();
      });
    });
  });
  QSTAMP(48);
  asm volatile("s_nop 7" : "+v"(a2[1][0]), "+v"(a2[1][1]), "+v"(a2[1][2]), "+v"(a2[1][3]), "+v"(a2[1][4]));
#pragma unroll
  for (int tau = 0; tau < 5; ++tau) red[(w * 20 + 15 + tau) * 64 + l] = a2[1][tau];
  __syncthreads();
#pragma unroll
  for (int tau = 0; tau < 5; ++tau) {
    const int i = (w * 5 + tau) * 64 + l;
    const f4 v = (red[i] + red[i + 1280]) + (red[i + 2560] + red[i + 3840]);
    const f4 o4 = v * gv[tau] + unbf4(xw[tau]);
    const u2 o = {pkbf(o4[0], o4[1]), pkbf(o4[2], o4[3])};
    *(u2*)(out + (size_t)tok * D + 80 * db + 16 * tau + 4 * g) = o;
  }
  STAMPW(3, 7);
  QFLUSH;
}

extern "C" __global__ void kz_end() {}

extern "C" void kernel_launch(void* const* d_in, const int* in_sizes, int n_in, void* d_out, int out_size, void* d_ws, size_t ws_size,
                              hipStream_t stream) {
  const u16* x = (const u16*)d_in[0];
  const Wts p = {(const u16*)d_in[4], (const u16*)d_in[5], (const u16*)d_in[6], (const u16*)d_in[1], (const u16*)d_in[2]};
  u8* ws = (u8*)d_ws;
  k0_setup<<<K0_GRID, 256, 0, stream>>>(x, (const u16*)d_in[7], (const u16*)d_in[8], ws);
  kmain<<<256, 256, 0, stream>>>(x, p, ws, (u16*)d_out);
}
```
